```python
import torch

def _softmax(x, axis=-1):
    return torch.softmax(x, dim=axis)
def _gelu(x):
    return torch.nn.functional.gelu(x, approximate="tanh")
def _silu(x):
    return torch.nn.functional.silu(x)
def _sigmoid(x):
    return torch.sigmoid(x)
def _layernorm(x, axis=-1, eps=1e-5):
    m = x.mean(dim=axis, keepdim=True)
    v = x.var(dim=axis, keepdim=True, unbiased=False)
    return (x - m) / torch.sqrt(v + eps)
def _scatter_add(tgt, idx, src, axis=0):
    return tgt.clone().index_add_(axis, idx, src)
def _segment_sum(src, seg, n):
    out = torch.zeros((n,) + src.shape[1:], dtype=src.dtype)
    return out.index_add_(0, seg, src)
def _pad(x, pw):
    flat = tuple(v for pair in reversed(pw) for v in pair)
    return torch.nn.functional.pad(x, flat)

kernel_name = 'concat_heads__linear_attn_block__ssm_conv_s4d__prompt'

def setup_inputs():

    _w = (lambda a: (lambda t: t.to(torch.bfloat16) if t.is_floating_point() else t)(torch.as_tensor(a)))
    def _A_setup():
        B, S, D = 1, 2048, 512
        return {
            "x": _w((lambda *s: torch.randn(*s, dtype=torch.bfloat16) * 0.05)(B, S, D)),
            "wqkv": _w((lambda *s: torch.randn(*s, dtype=torch.bfloat16) * 0.4)(D, 3 * D)),
            "wo": _w((lambda *s: torch.randn(*s, dtype=torch.bfloat16) * 0.05)(D, D)),
        }
    _A_in = _A_setup()

    def _B_setup():
        Bsz, L, D, N = 1, 2048, 512, 128
        return {
            "u": _w((lambda *s: torch.randn(*s, dtype=torch.bfloat16) * 0.05)(Bsz, L, D)),
            "log_A_real": _w((lambda *s: torch.randn(*s, dtype=torch.bfloat16) * 6.4)(D, N) * 0.5),
            "A_imag": _w((lambda *s: torch.randn(*s, dtype=torch.bfloat16) * 0.05)(D, N) * 3.14),
            "C_real": _w((lambda *s: torch.randn(*s, dtype=torch.bfloat16) * 0.1)(D, N) / N ** 0.5),
            "C_imag": _w((lambda *s: torch.randn(*s, dtype=torch.bfloat16) * 0.1)(D, N) / N ** 0.5),
            "log_dt": _w((lambda *s: torch.randn(*s, dtype=torch.bfloat16) * 0.05)(D) * 0.5 - 4.0),
            "D_skip": _w((lambda *s: torch.randn(*s, dtype=torch.bfloat16) * 0.05)(D)),
        }
    _B_in = _B_setup()

    return {
        "A_x": _A_in["x"],
        "A_wqkv": _A_in["wqkv"],
        "A_wo": _A_in["wo"],
        "B_u": _B_in["u"],
        "B_log_A_real": _B_in["log_A_real"],
        "B_A_imag": _B_in["A_imag"],
        "B_C_real": _B_in["C_real"],
        "B_C_imag": _B_in["C_imag"],
        "B_log_dt": _B_in["log_dt"],
        "B_D_skip": _B_in["D_skip"],
    }

def reference(A_x, A_wqkv, A_wo, B_u, B_log_A_real, B_A_imag, B_C_real, B_C_imag, B_log_dt, B_D_skip):
    def _A_ref(x, wqkv, wo):
        B, S, D = x.shape
        H = 32; Dh = D // H
        qkv = (lambda x, *d: x.permute(*d))((x @ wqkv).reshape(B, S, 3, H, Dh), 2, 0, 3, 1, 4)
        q, k, v = qkv[0], qkv[1], qkv[2]
        phi = lambda t: torch.where(t > 0, t + 1, torch.exp(t)) + 1e-6
        pq, pk = phi(q), phi(k)
        kv = torch.einsum("bhsd,bhse->bhde", pk, v)
        z = torch.einsum("bhsd->bhd", pk)
        num = torch.einsum("bhqd,bhde->bhqe", pq, kv)
        den = torch.einsum("bhqd,bhd->bhq", pq, z)[..., None] + 1e-6
        o = num / den
        return (lambda x, *d: x.permute(*d))(o, 0, 2, 1, 3).reshape(B, S, D) @ wo

    def _B_ref(u, log_A_real, A_imag, C_real, C_imag, log_dt, D_skip):
        L = u.shape[1]
        dt = torch.exp(log_dt)
        A = -torch.exp(log_A_real) + 1j * A_imag
        C = (C_real + 1j * C_imag) * dt[:, None]
        pos = (lambda x, dt: x.to(dt))(torch.arange(L), log_dt.dtype)
        P = torch.exp((A * dt[:, None])[:, :, None] * pos[None, None, :])
        K = 2.0 * torch.einsum("dn,dnl->dl", C, P).real
        L2 = 2 * L
        u_t = (lambda x, *d: x.permute(*d))(u, 0, 2, 1)
        Kf = (lambda x, *a, **k: torch.fft.rfft(x.float(), *a, **k))(K, L2)
        Uf = (lambda x, *a, **k: torch.fft.rfft(x.float(), *a, **k))(u_t, L2)
        y = (lambda x, *a, **k: torch.fft.irfft(x, *a, **k).to(torch.bfloat16))(Uf * Kf[None, :, :], L2)[:, :, :L]
        y = (lambda x, *d: x.permute(*d))(y, 0, 2, 1)
        return y + u * D_skip[None, None, :]

    a = _A_ref(A_x, A_wqkv, A_wo)
    b = _B_ref(B_u, B_log_A_real, B_A_imag, B_C_real, B_C_imag, B_log_dt, B_D_skip)
    return torch.concatenate([a, b], axis=-1)
```

```cpp
#include <hip/hip_runtime.h>
#include <hip/hip_bf16.h>
typedef __hip_bfloat16 bf16;
__device__ inline float b2f(bf16 x){ return __bfloat162float(x);}
__device__ inline float rb(float x){ return __bfloat162float(__float2bfloat16(x)); }

__global__ void k_qkv(const bf16* x, const bf16* w, float* qkv){
  int c = blockIdx.x*256+threadIdx.x; int s = blockIdx.y;
  float acc=0; for(int k=0;k<512;k++) acc += b2f(x[s*512+k])*b2f(w[k*1536+c]);
  acc = rb(acc);
  if(c<1024){ float t = acc>0 ? rb(acc+1.f) : rb(expf(acc)); acc = rb(t+1e-6f);}
  qkv[s*1536+c]=acc;
}
__global__ void k_kv(const float* qkv, float* kv){
  int h=blockIdx.x, t=threadIdx.x; float acc=0;
  if(t<256){int d=t>>4,e=t&15; for(int s=0;s<2048;s++) acc+=qkv[s*1536+512+h*16+d]*qkv[s*1536+1024+h*16+e];}
  else {int d=t-256; for(int s=0;s<2048;s++) acc+=qkv[s*1536+512+h*16+d];}
  kv[h*272+t]=rb(acc);
}
__global__ void k_o(const float* qkv, const float* kv, float* o){
  int c=blockIdx.x*256+threadIdx.x; int s=blockIdx.y; int h=c>>4,e=c&15;
  float num=0,den=0; for(int d=0;d<16;d++){float p=qkv[s*1536+h*16+d]; num+=p*kv[h*272+d*16+e]; den+=p*kv[h*272+256+d];}
  num=rb(num); den=rb(rb(den)+1e-6f); o[s*512+c]=rb(num/den);
}
__global__ void k_out(const float* o, const bf16* wo, bf16* out){
  int c=blockIdx.x*256+threadIdx.x; int s=blockIdx.y; float acc=0;
  for(int k=0;k<512;k++) acc+=o[s*512+k]*b2f(wo[k*512+c]);
  out[s*1024+c]=__float2bfloat16(acc);
}
__global__ void k_kgen(const bf16* lar,const bf16* aim,const bf16* cr,const bf16* ci,const bf16* ldt,float* K){
  int l=blockIdx.x*256+threadIdx.x; int d=blockIdx.y;
  float dt=rb(expf(b2f(ldt[d]))); float pos=rb((float)l); float acc=0;
  for(int n=0;n<128;n++){ float ar=-rb(expf(b2f(lar[d*128+n])))*dt, ai=b2f(aim[d*128+n])*dt;
    float c_r=b2f(cr[d*128+n])*dt, c_i=b2f(ci[d*128+n])*dt; float e=expf(ar*pos); float sn,cs; sincosf(ai*pos,&sn,&cs);
    acc += c_r*e*cs - c_i*e*sn; }
  K[d*2048+l]=2.f*acc;
}
__global__ void k_conv(const bf16* u,const float* K,const bf16* dsk,bf16* out){
  int d=blockIdx.x*256+threadIdx.x; int l=blockIdx.y; float acc=0;
  for(int j=0;j<=l;j++) acc+=K[d*2048+j]*b2f(u[(l-j)*512+d]);
  float y=rb(acc); float sk=rb(b2f(u[l*512+d])*b2f(dsk[d]));
  out[l*1024+512+d]=__float2bfloat16(y+sk);
}
extern "C" void kernel_launch(void* const* d_in, const int* in_sizes, int n_in, void* d_out, int out_size, void* d_ws, size_t ws_size, hipStream_t stream){
  const bf16* x=(const bf16*)d_in[0]; const bf16* wqkv=(const bf16*)d_in[1]; const bf16* wo=(const bf16*)d_in[2];
  const bf16* u=(const bf16*)d_in[3];
  float* qkv=(float*)d_ws; float* kv=qkv+2048*1536; float* o=kv+32*272; float* K=o+2048*512;
  bf16* out=(bf16*)d_out;
  k_qkv<<<dim3(6,2048),256,0,stream>>>(x,wqkv,qkv);
  k_kv<<<32,272,0,stream>>>(qkv,kv);
  k_o<<<dim3(2,2048),256,0,stream>>>(qkv,kv,o);
  k_out<<<dim3(2,2048),256,0,stream>>>(o,wo,out);
  k_kgen<<<dim3(8,512),256,0,stream>>>((const bf16*)d_in[4],(const bf16*)d_in[5],(const bf16*)d_in[6],(const bf16*)d_in[7],(const bf16*)d_in[8],K);
  k_conv<<<dim3(2,2048),256,0,stream>>>(u,K,(const bf16*)d_in[9],out);
}
```

```cpp
#include <hip/hip_runtime.h>
#include <hip/hip_bf16.h>

typedef unsigned short u16;
typedef unsigned int u32;
typedef unsigned long long u64;
typedef short bf16x8 __attribute__((ext_vector_type(8)));
typedef short bf16x4 __attribute__((ext_vector_type(4)));
typedef float f32x4 __attribute__((ext_vector_type(4)));
typedef float f32x2 __attribute__((ext_vector_type(2)));
typedef u32 u32x4 __attribute__((ext_vector_type(4)));
typedef u32 u32x2 __attribute__((ext_vector_type(2)));
typedef __bf16 bfv2 __attribute__((ext_vector_type(2)));
#define GAS __attribute__((address_space(1)))
#define LAS __attribute__((address_space(3)))
#define DI __device__ __forceinline__
#define RLX_AGENT __ATOMIC_RELAXED, __HIP_MEMORY_SCOPE_AGENT

constexpr size_t WS_EPOCH = 0;
constexpr size_t WS_FIN   = 8192;
constexpr size_t WS_FINZ  = WS_FIN + 16384;
constexpr size_t WS_PART  = 65536;
constexpr size_t WS_PQ    = 2u << 20;
constexpr size_t WS_GU    = 4u << 20;
constexpr size_t WS_YT    = 8u << 20;
constexpr size_t WS_DBG   = 16u << 20;
constexpr int SPIN_CAP = 4096;

struct Params {
  const u16 *x, *wqkv, *wo, *u, *lar, *aim, *cr, *ci, *ldt, *dsk;
  u16* out;
  char* ws;
};

#define STAMP_AT(k, i) do {} while (0)

DI float bf2f(u32 h) { return __uint_as_float(h << 16); }
DI u32 pk2(float lo, float hi) { bfv2 v = __builtin_convertvector((f32x2){lo, hi}, bfv2); return __builtin_bit_cast(u32, v); }
DI float rbf(float x) { return bf2f(pk2(x, x) & 0xffffu); }
DI f32x4 mfma32(bf16x8 a, bf16x8 b, f32x4 c) { return __builtin_amdgcn_mfma_f32_16x16x32_bf16(a, b, c, 0, 0, 0); }
DI f32x4 mfma16(bf16x4 a, bf16x4 b, f32x4 c) { return __builtin_amdgcn_mfma_f32_16x16x16bf16_1k(a, b, c, 0, 0, 0); }
DI bf16x8 as8(u32x4 v) { return __builtin_bit_cast(bf16x8, v); }
DI bf16x4 as4(u32x2 v) { return __builtin_bit_cast(bf16x4, v); }
DI u32 rfl(u32 v) { return __builtin_amdgcn_readfirstlane(v); }
DI void dma16(u32 ldsaddr, const void* sbase, u32 voff) {
  u32 keep;
  asm volatile("s_mov_b32 %0, m0\n\ts_mov_b32 m0, %1\n\ts_nop 0\n\tglobal_load_lds_dwordx4 %2, %3\n\ts_mov_b32 m0, %0"
               : "=&s"(keep) : "s"(ldsaddr), "v"(voff), "s"(sbase) : "memory");
}
#define WAIT_VM(n) asm volatile("s_waitcnt vmcnt(" #n ")" ::: "memory")
#define WAIT_LGKM0() asm volatile("s_waitcnt lgkmcnt(0)" ::: "memory")
#define BAR() do { asm volatile("" ::: "memory"); __builtin_amdgcn_s_barrier(); asm volatile("" ::: "memory"); } while (0)
#define LBAR() do { WAIT_LGKM0(); BAR(); } while (0)
DI __amdgpu_buffer_rsrc_t mkrsrc(const void* base, u32 bytes) { return __builtin_amdgcn_make_buffer_rsrc((void*)base, 0, bytes, 0x00020000); }
DI void st16_dev(__amdgpu_buffer_rsrc_t r, u32 off, u32x4 v) { __builtin_amdgcn_raw_buffer_store_b128(v, r, off, 0, 16); }
DI u32x4 ld16_dev(__amdgpu_buffer_rsrc_t r, u32 off) { return __builtin_amdgcn_raw_buffer_load_b128(r, off, 0, 16); }
DI bf16x4 trread(LAS char* a) { return __builtin_amdgcn_ds_read_tr16_b64_v4i16((LAS bf16x4*)a); }
DI bf16x8 cat8(bf16x4 a, bf16x4 b) { return __builtin_shufflevector(a, b, 0, 1, 2, 3, 4, 5, 6, 7); }
DI float phi(float t) { float e = __builtin_amdgcn_exp2f(t * 1.44269504088896f); return (t > 0.f ? t + 1.f : e) + 1e-6f; }
DI f32x2 cmul(f32x2 x, f32x2 w, f32x2 wn) {
  f32x2 t = x * (f32x2){w.x, w.x};
  return __builtin_elementwise_fma((f32x2){x.y, x.x}, wn, t);
}

DI void roleA1(const Params& p, LAS char* sm) {
  const int a = blockIdx.x, xcd = a & 7, idx = a >> 3, rbl = idx & 7, cgl = idx >> 3;
  const int rb = (xcd >> 1) * 8 + rbl, cg = (xcd & 1) * 4 + cgl;
  const int w = rfl(threadIdx.x >> 6), l = threadIdx.x & 63, g = l >> 4, e = l & 15;
  const int head = cg * 4 + w;
  const u32 smb = (u32)(size_t)sm;
  __builtin_amdgcn_s_setprio(2);
  STAMP_AT(0, 0);
  const u32 vx = (u32)((l >> 3) * 1024 + (((l & 7) ^ (l >> 3)) * 16));
  const u32 fq = (u32)((l >> 4) & 1);
  const u32 vw0 = (u32)((l >> 3) * 3072 + (((l & 7) ^ (fq << 1)) * 16));
  const u32 vw1 = (u32)((l >> 3) * 3072 + (((l & 7) ^ ((fq | 2) << 1)) * 16));
  const char* xb = (const char*)p.x + (size_t)(rb * 64 + w * 16) * 1024;
  const char* wb = (const char*)p.wqkv + (size_t)(w * 16) * 3072 + cg * 128;
  const int s0 = (rbl + 2 * cgl) & 7;
  auto stage = [&](int i) {
    const int s = (s0 + i) & 7; const u32 buf = smb + (i & 1) * 32768;
    const char* xs = xb + s * 128; const char* wsx = wb + (size_t)s * 64 * 3072;
    dma16(buf + w * 2048, xs, vx);
    dma16(buf + w * 2048 + 1024, xs + 8192, vx);
#pragma unroll
    for (int pt = 0; pt < 3; ++pt) {
      dma16(buf + 8192 + pt * 8192 + w * 2048, wsx + pt * 1024, vw0);
      dma16(buf + 8192 + pt * 8192 + w * 2048 + 1024, wsx + pt * 1024 + 8 * 3072, vw1);
    }
  };
  stage(0); stage(1);
  const u32 epoch = __hip_atomic_load((GAS u32*)(p.ws + WS_EPOCH + cg * 256), RLX_AGENT);
  const int xo0 = e * 128 + ((g ^ (l & 7)) * 16), xo1 = xo0 ^ 64;
  const int q4 = e >> 2, pp = e & 3;
  const int wo_ = 8192 + (8 * g + q4) * 128 + ((w ^ (((q4 >> 1) & 1) | ((g & 1) << 1))) * 32) + pp * 8;
  f32x4 aq[4], ak[4], av[4];
#pragma unroll
  for (int t = 0; t < 4; ++t) { aq[t] = (f32x4){0, 0, 0, 0}; ak[t] = aq[t]; av[t] = aq[t]; }
#pragma unroll 2
  for (int i = 0; i < 8; ++i) {
    if (i == 0) WAIT_VM(9); else if (i < 7) WAIT_VM(8); else WAIT_VM(0);
    BAR();
    LAS char* buf = sm + (i & 1) * 32768;
    bf16x8 xf[2][4], wf[2][3];
#pragma unroll
    for (int ks = 0; ks < 2; ++ks) {
#pragma unroll
      for (int t = 0; t < 4; ++t) xf[ks][t] = *(LAS bf16x8*)(buf + (ks ? xo1 : xo0) + t * 2048);
#pragma unroll
      for (int pt = 0; pt < 3; ++pt) wf[ks][pt] = cat8(trread(buf + wo_ + pt * 8192 + ks * 4096), trread(buf + wo_ + pt * 8192 + ks * 4096 + 512));
    }
    WAIT_LGKM0();
    __builtin_amdgcn_sched_barrier(0);
    BAR();
    if (i < 6) stage(i + 2);
    __builtin_amdgcn_sched_barrier(0);
#pragma unroll
    for (int ks = 0; ks < 2; ++ks)
#pragma unroll
      for (int t = 0; t < 4; ++t) {
        ak[t] = mfma32(xf[ks][t], wf[ks][1], ak[t]);
        av[t] = mfma32(xf[ks][t], wf[ks][2], av[t]);
        aq[t] = mfma32(wf[ks][0], xf[ks][t], aq[t]);
      }
  }
  STAMP_AT(0, 1);
  const u32 tag = rfl(epoch) + 1u;
  f32x4 kv = {0, 0, 0, 0}, zz = {0, 0, 0, 0};
  const u32x4 ones = {0x3f803f80u, 0x3f803f80u, 0x3f803f80u, 0x3f803f80u};
#pragma unroll
  for (int h = 0; h < 2; ++h) {
    u32x4 kf = {pk2(phi(ak[2 * h][0]), phi(ak[2 * h][1])), pk2(phi(ak[2 * h][2]), phi(ak[2 * h][3])),
                pk2(phi(ak[2 * h + 1][0]), phi(ak[2 * h + 1][1])), pk2(phi(ak[2 * h + 1][2]), phi(ak[2 * h + 1][3]))};
    u32x4 vf = {pk2(av[2 * h][0], av[2 * h][1]), pk2(av[2 * h][2], av[2 * h][3]),
                pk2(av[2 * h + 1][0], av[2 * h + 1][1]), pk2(av[2 * h + 1][2], av[2 * h + 1][3])};
    kv = mfma32(as8(kf), as8(vf), kv);
    zz = mfma32(as8(kf), as8(ones), zz);
  }
  const __amdgpu_buffer_rsrc_t rpart = mkrsrc(p.ws + WS_PART, 32 * 9 * 4096);
  st16_dev(rpart, (u32)(((head * 9 + (l >> 3)) * 32 + rb) * 128 + (l & 7) * 16), (u32x4){pk2(kv[0], kv[1]), tag, pk2(kv[2], kv[3]), tag});
  if (e < 2) st16_dev(rpart, (u32)(((head * 9 + 8) * 32 + rb) * 128 + (e * 4 + g) * 16), (u32x4){pk2(zz[0], zz[1]), tag, pk2(zz[2], zz[3]), tag});
  STAMP_AT(0, 2);
  {
    char* pq = p.ws + WS_PQ + ((size_t)(rb * 4) * 16 + (head >> 1)) * 1024 + l * 16 + (head & 1) * 8;
#pragma unroll
    for (int t = 0; t < 4; ++t)
      *(u32x2*)(pq + t * 16384) = (u32x2){pk2(phi(aq[t][0]), phi(aq[t][1])), pk2(phi(aq[t][2]), phi(aq[t][3]))};
  }
  STAMP_AT(0, 3);
  if (rb < 9) {
    const u32 off = (u32)((head * 9 + rb) * 4096 + l * 16);
    u32x4 v[4];
    for (int spin = 0; spin < SPIN_CAP; ++spin) {
      bool ok = true;
      asm volatile("" ::: "memory");
#pragma unroll
      for (int i = 0; i < 4; ++i) { v[i] = ld16_dev(rpart, off + i * 1024); ok &= (v[i][1] == tag) & (v[i][3] == tag); }
      if (__all(ok)) break;
      __builtin_amdgcn_s_sleep(1);
    }
    float s[4] = {0.f, 0.f, 0.f, 0.f};
#pragma unroll
    for (int i = 0; i < 4; ++i) {
      s[0] += bf2f(v[i][0] & 0xffffu); s[1] += __uint_as_float(v[i][0] & 0xffff0000u);
      s[2] += bf2f(v[i][2] & 0xffffu); s[3] += __uint_as_float(v[i][2] & 0xffff0000u);
    }
#pragma unroll
    for (int j = 0; j < 4; ++j) { s[j] += __shfl_xor(s[j], 8); s[j] += __shfl_xor(s[j], 16); s[j] += __shfl_xor(s[j], 32); }
    const u32x2 r = {pk2(s[0], s[1]), pk2(s[2], s[3])};
    if (rb < 8) { if (l < 8) *(u32x2*)(p.ws + WS_FIN + (head >> 1) * 1024 + (rb * 8 + l) * 16 + (head & 1) * 8) = r; }
    else if (l < 4) *(u32x2*)(p.ws + WS_FINZ + head * 32 + l * 8) = r;
    if (rb == 8) { BAR(); if (threadIdx.x == 0) __hip_atomic_store((GAS u32*)(p.ws + WS_EPOCH + cg * 256), tag, RLX_AGENT); }
  }
  STAMP_AT(0, 4);
}

constexpr int B_SCR = 0;
constexpr int B_UT = 20480;
constexpr int B_KX = 29696;
constexpr int B_UC = 32768;
constexpr int UC_S = 4160;
constexpr int B_KA = 49664;
constexpr int B_PX = 58880;
DI void roleB1(const Params& p, LAS char* sm) {
  const int b = blockIdx.x - 256, gch = b & 7, bi = b >> 3;
  const int t = threadIdx.x, w = rfl(t >> 6), l = t & 63, g = l >> 4, e = l & 15;
  const int chl = w >> 1, mh = w & 1, ch = gch * 64 + bi * 2 + chl;
  STAMP_AT(0, 0);
  const char* ut = (const char*)p.u + (size_t)(bi * 64 + (t >> 3)) * 1024 + gch * 128 + (t & 7) * 16;
  const u32x4 u0 = *(const u32x4*)ut, u1 = *(const u32x4*)(ut + 32 * 1024);
  const u32 epoch = __hip_atomic_load((GAS u32*)(p.ws + WS_EPOCH + (8 + gch) * 256), RLX_AGENT);
  const int pi = ch * 128 + mh * 64 + l;
  const u32 r_lar = p.lar[pi], r_aim = p.aim[pi], r_cr = p.cr[pi], r_ci = p.ci[pi], r_ldt = p.ldt[ch];
  if (t < 64) *(LAS u32x4*)(sm + B_KA + (t >> 5) * 4608 + (t & 31) * 16) = (u32x4){0, 0, 0, 0};
  else if (t < 96) *(LAS u32*)(sm + B_UC + ((t - 64) >> 3) * UC_S + (t & 7) * 4) = 0u;
  *(LAS u32x4*)(sm + B_UT + (t >> 3) * 144 + (t & 7) * 16) = u0;
  *(LAS u32x4*)(sm + B_UT + ((t >> 3) + 32) * 144 + (t & 7) * 16) = u1;
  const u32 tag = rfl(epoch) + 1u;
  asm volatile("" :: "s"(tag) : "memory");
  LBAR();
  const __amdgpu_buffer_rsrc_t rgu = mkrsrc(p.ws + WS_GU, 512 * 8192);
  {
    const int cq = w * 4 + g;
    u32x2 r[4];
#pragma unroll
    for (int j = 0; j < 4; ++j) r[j] = *(LAS u32x2*)(sm + B_UT + (e * 4 + j) * 144 + cq * 8);
    const u32 off = (u32)((gch * 64 + cq * 4) * 8192 + (bi * 16 + e) * 16);
#pragma unroll
    for (int c = 0; c < 4; ++c) {
      const u32 sel = (c & 1) ? 0x07060302u : 0x05040100u;
      const u32 d0 = __builtin_amdgcn_perm(r[1][c >> 1], r[0][c >> 1], sel), d1 = __builtin_amdgcn_perm(r[3][c >> 1], r[2][c >> 1], sel);
      st16_dev(rgu, off + c * 8192, (u32x4){d0, tag, d1, tag});
    }
  }
  STAMP_AT(0, 1);
  const float dt = rbf(expf(bf2f(r_ldt)));
  const float a2 = -rbf(expf(bf2f(r_lar))) * dt * 1.44269504088896f;
  const float th = bf2f(r_aim) * dt * 0.15915494309189535f;
  auto wpow = [&](float s) -> f32x2 {
    const float m = __builtin_amdgcn_exp2f(a2 * s), f = __builtin_amdgcn_fractf(th * s);
    return (f32x2){m * __builtin_amdgcn_cosf(f), m * __builtin_amdgcn_sinf(f)};
  };
  const f32x2 w1 = wpow(1.f), w8 = wpow(8.f), w128 = wpow(128.f);
  const f32x2 w1n = {-w1.y, w1.y}, w128n = {-w128.y, w128.y}, w8cn = {w8.y, -w8.y};
  LAS char* scr = sm + B_SCR + w * 4624;
  LAS u32* sw = (LAS u32*)(scr + l * 4);
  auto put = [&](int row, f32x2 v) { sw[row * 68] = pk2(v.x, v.y); };
  const int fo = e * 272 + g * 16;
  bf16x8 cf[4], cf2[4];
  {
    f32x2 c = {1.f, 0.f};
    put(0, c);
#pragma unroll
    for (int i = 1; i <= 16; ++i) { c = cmul(c, w8, w8cn); put(i, c); }
  }
  __builtin_amdgcn_wave_barrier(); asm volatile("" ::: "memory");
#pragma unroll
  for (int ks = 0; ks < 4; ++ks) { cf[ks] = *(LAS bf16x8*)(scr + fo + ks * 64); cf2[ks] = *(LAS bf16x8*)(scr + fo + 272 + ks * 64); }
  __builtin_amdgcn_wave_barrier(); asm volatile("" ::: "memory");
  f32x2 G[8];
  G[0] = (f32x2){2.f * bf2f(r_cr) * dt, 2.f * bf2f(r_ci) * dt};
#pragma unroll
  for (int i = 1; i < 8; ++i) G[i] = cmul(G[i - 1], w1, w1n);
#pragma unroll
  for (int i = 0; i < 8; ++i) put(i, G[i]);
#pragma unroll
  for (int i = 0; i < 8; ++i) { G[i] = cmul(G[i], w128, w128n); put(8 + i, G[i]); }
  f32x4 d0 = {0, 0, 0, 0}, d1 = d0, d1s = d0, d2 = d0, d2s = d0;
  __builtin_amdgcn_wave_barrier(); asm volatile("" ::: "memory");
#pragma unroll
  for (int ks = 0; ks < 4; ++ks) d0 = mfma32(*(LAS bf16x8*)(scr + fo + ks * 64), cf[ks], d0);
  __builtin_amdgcn_wave_barrier(); asm volatile("" ::: "memory");
#pragma unroll
  for (int h = 0; h < 2; ++h)
#pragma unroll
    for (int i = 0; i < 4; ++i) { G[2 * i] = cmul(G[2 * i], w128, w128n); put(4 * h + i, G[2 * i]); }
#pragma unroll
  for (int h = 0; h < 4; ++h)
#pragma unroll
    for (int i = 0; i < 2; ++i) { G[4 * i] = cmul(G[4 * i], w128, w128n); put(8 + 2 * h + i, G[4 * i]); }
  __builtin_amdgcn_wave_barrier(); asm volatile("" ::: "memory");
#pragma unroll
  for (int ks = 0; ks < 4; ++ks) { const bf16x8 af = *(LAS bf16x8*)(scr + fo + ks * 64); d1 = mfma32(af, cf[ks], d1); d1s = mfma32(af, cf2[ks], d1s); }
  __builtin_amdgcn_wave_barrier(); asm volatile("" ::: "memory");
#pragma unroll
  for (int h = 0; h < 8; ++h) { G[0] = cmul(G[0], w128, w128n); put(4 * (h >> 1) + (h & 1), G[0]); }
  __builtin_amdgcn_wave_barrier(); asm volatile("" ::: "memory");
#pragma unroll
  for (int ks = 0; ks < 4; ++ks) { const bf16x8 af = *(LAS bf16x8*)(scr + fo + ks * 64); d2 = mfma32(af, cf[ks], d2); d2s = mfma32(af, cf2[ks], d2s); }
  {
    const f32x4 pa = {d2[0], d2[1], d2s[0], d2s[1]}, pb = {d1s[0], d1s[2], 0.f, 0.f};
    LAS f32x4* px = (LAS f32x4*)(sm + B_PX + w * 2048 + l * 16);
    px[0] = mh ? d0 : d1; px[64] = mh ? pa : pb;
    STAMP_AT(0, 2);
    const u32 off = (u32)(ch * 8192 + (mh * 256 + l) * 16);
    u32x4 v[4], vp;
    for (int spin = 0; spin < SPIN_CAP; ++spin) {
      bool ok = true;
      asm volatile("" ::: "memory");
#pragma unroll
      for (int i = 0; i < 4; ++i) { v[i] = ld16_dev(rgu, off + i * 1024); ok &= (v[i][1] == tag) & (v[i][3] == tag); }
      vp = ld16_dev(rgu, (u32)(ch * 8192 + 255 * 16)); ok &= (vp[3] == tag);
      if (__all(ok)) break;
      __builtin_amdgcn_s_sleep(1);
    }
    STAMP_AT(0, 3);
    LAS char* uc = sm + B_UC + chl * 2 * UC_S + 4 + 28 + (mh * 256 + l) * 8;
    u32 carry = mh ? vp[2] : 0u;
#pragma unroll
    for (int i = 0; i < 4; ++i) {
      const u32 prev = __builtin_amdgcn_update_dpp(carry, v[i][2], 0x138  , 0xf, 0xf, false);
      carry = __builtin_amdgcn_readlane(v[i][2], 63);
      *(LAS u32x2*)(uc + UC_S + i * 512) = (u32x2){v[i][0], v[i][2]};
      *(LAS u32x2*)(uc + i * 512) = (u32x2){__builtin_amdgcn_alignbit(v[i][0], prev, 16), __builtin_amdgcn_alignbit(v[i][2], v[i][0], 16)};
    }
    if (mh && l == 63) *(LAS u32*)(uc + 3 * 512 + 8) = v[3][2] >> 16;
    LBAR();
    if (bi == 0 && t == 0) __hip_atomic_store((GAS u32*)(p.ws + WS_EPOCH + (8 + gch) * 256), tag, RLX_AGENT);
    LAS f32x4* py = (LAS f32x4*)(sm + B_PX + (w ^ 1) * 2048 + l * 16);
    const f32x4 qa = py[0], qb = py[64];
    LAS char* ka = sm + B_KA + chl * 4608 + 512 + e * 16;
    if (mh == 0) {
      const f32x4 k0 = d0 + qa, k2 = pa + qb;
      *(LAS u32x2*)(ka + (g >> 1) * 256 + ((g & 1) ^ 1) * 8) = (u32x2){pk2(k0[3], k0[2]), pk2(k0[1], k0[0])};
#pragma unroll
      for (int r = 0; r < 2; ++r) {
        const float v_ = k2[r], w_ = k2[2 + r], m_ = (e & 1) ? w_ : v_;
        const u32 vv = pk2(v_, v_);
        *(LAS u32x4*)(ka + 2048 + (2 * g + r) * 256) = (u32x4){pk2(w_, w_), pk2(w_, m_), vv, vv};
      }
    } else {
      const f32x4 k1 = d1 + qa, k1s = pb + qb;
      const bool lo = g < 2;
      const float x_ = lo ? k1[2] : k1[1];
      *(LAS u32x4*)(ka + (lo ? 512 + g * 256 : 1024 + (g - 2) * 512)) =
          (u32x4){pk2(k1s[0], lo ? k1[3] : k1s[0]), pk2(x_, x_), pk2(x_, lo ? k1[1] : k1[0]), pk2(k1[0], k1[0])};
      if (!lo) *(LAS u32x4*)(ka + 1024 + (g - 2) * 512 + 256) = (u32x4){pk2(k1s[1], k1s[1]), pk2(k1[3], k1[3]), pk2(k1[3], k1[2]), pk2(k1[2], k1[2])};
    }
    LBAR();
  }
  STAMP_AT(0, 4);
  f32x4 acc[8];
#pragma unroll
  for (int c = 0; c < 8; ++c) acc[c] = (f32x4){0, 0, 0, 0};
  {
    LAS char* tb = sm + B_UC + (chl * 2 + (e & 1)) * UC_S + 4 + (16 * (g >> 1) - 8 * (g & 1) + 8 + (e & ~1)) * 2 + mh * 256;
    LAS char* vb = sm + B_KA + chl * 4608 + 512 + (e - (g >> 1)) * 32 + (g & 1) * 16 - mh * 256;
#pragma unroll 2
    for (int ee = 0; ee < 4; ++ee) {
      bf16x8 T[8], V[8];
#pragma unroll
      for (int m = 0; m < 8; ++m) {
        LAS u32* tp = (LAS u32*)(tb + ee * 64 + m * 512);
        T[m] = as8((u32x4){tp[0], tp[1], tp[2], tp[3]});
        V[m] = *(LAS bf16x8*)(vb - ee * 64 + m * 512);
      }
#pragma unroll
      for (int m = 0; m < 8; ++m)
#pragma unroll
        for (int m2 = 0; m2 + m < 8; ++m2) acc[m + m2] = mfma32(V[m2], T[m], acc[m + m2]);
    }
  }
  STAMP_AT(0, 5);
#pragma unroll
  for (int c = 0; c < 8; ++c) *(LAS f32x4*)(sm + B_SCR + (w * 8 + c) * 1024 + l * 16) = acc[c];
  LBAR();
#pragma unroll
  for (int k = 0; k < 2; ++k) {
    const int c = 2 * w + k;
    LAS f32x4* xp = (LAS f32x4*)(sm + B_SCR + c * 1024 + l * 16);
    const f32x4 y0 = xp[0] + xp[512], y1 = xp[1024] + xp[1536];
    *(u32x4*)(p.ws + WS_YT + (size_t)(((gch * 32 + 4 * c + g) * 32 + bi) * 256 + e * 16)) =
        (u32x4){pk2(y0[0], y1[0]), pk2(y0[1], y1[1]), pk2(y0[2], y1[2]), pk2(y0[3], y1[3])};
  }
  STAMP_AT(0, 6);
}

__global__ __launch_bounds__(256, 2) void k_one(Params p) {
  __shared__ __attribute__((aligned(16))) char smem[81920];
  if (blockIdx.x < 256) roleA1(p, (LAS char*)smem); else roleB1(p, (LAS char*)smem);
}

constexpr int C_WO = 0, C_KV = 65536, C_ZX = 81920, C_YT = 83968, C_ST = 93184, C_END = 93184 + 4 * 2304;
__global__ __launch_bounds__(256, 1) void k_two(Params p) {
  __shared__ __attribute__((aligned(16))) char smem[C_END];
  LAS char* sm = (LAS char*)smem;
  const int a = blockIdx.x, xcd = a & 7, idx = a >> 3;
  const int rb = (xcd >> 1) * 8 + (idx & 7), cg = (xcd & 1) * 4 + (idx >> 3);
  const int t = threadIdx.x, w = rfl(t >> 6), l = t & 63, g = l >> 4, e = l & 15;
  const u32 smb = (u32)(size_t)sm;
  STAMP_AT(1, 0);
  const char* yt = p.ws + WS_YT + (size_t)(xcd * 32 + idx) * 8192 + t * 16;
  const u32x4 y0 = *(const u32x4*)yt, y1 = *(const u32x4*)(yt + 4096);
  const char* ut = (const char*)p.u + (size_t)(idx * 64 + (t >> 3)) * 1024 + xcd * 128 + (t & 7) * 16;
  const u32x4 u0 = *(const u32x4*)ut, u1 = *(const u32x4*)(ut + 32 * 1024);
  const u32x4 dk = *(const u32x4*)((const char*)p.dsk + xcd * 128 + (t & 7) * 16);
  const u32x2 zr = *(const u32x2*)(p.ws + WS_FINZ + (t & 127) * 8);
#pragma unroll
  for (int i = 0; i < 4; ++i) dma16(smb + C_KV + (w * 4 + i) * 1024, p.ws + WS_FIN + (w * 4 + i) * 1024, (u32)(l * 16));
  u32x4 pq[16];
  {
    const char* pp_ = p.ws + WS_PQ + (size_t)((rb * 4 + w) * 16) * 1024 + l * 16;
#pragma unroll
    for (int i = 0; i < 16; ++i) pq[i] = *(const u32x4*)(pp_ + i * 1024);
  }
  {
    const u32 vw = (u32)((l >> 3) * 1024 + (((l & 7) ^ (((l >> 4) & 3) << 1)) * 16));
    const char* wb = (const char*)p.wo + cg * 128 + (size_t)w * 8 * 1024;
#pragma unroll
    for (int i = 0; i < 16; ++i) dma16(smb + C_WO + (i * 4 + w) * 1024, wb + (size_t)i * 32 * 1024, vw);
  }
  if (t < 128) {
    const int hd = t >> 2;
    *(LAS u32x4*)(sm + C_ZX + hd * 64 + (t & 3) * 16) = (hd & 1) ? (u32x4){0, 0, zr[0], zr[1]} : (u32x4){zr[0], zr[1], 0, 0};
  }
#pragma unroll
  for (int r = 0; r < 4; ++r) {
    *(LAS u32*)(sm + C_YT + (16 * r + e) * 144 + (t >> 4) * 4) = y0[r];
    *(LAS u32*)(sm + C_YT + (16 * r + e) * 144 + ((t >> 4) + 16) * 4) = y1[r];
  }
  LBAR();
#pragma unroll
  for (int k = 0; k < 2; ++k) {
    const u32x4 yv = *(LAS u32x4*)(sm + C_YT + ((t >> 3) + 32 * k) * 144 + (t & 7) * 16);
    const u32x4 uv = k ? u1 : u0;
    u32x4 o;
#pragma unroll
    for (int j = 0; j < 4; ++j) {
      const u32 sk = pk2(bf2f(uv[j] & 0xffffu) * bf2f(dk[j] & 0xffffu), __uint_as_float(uv[j] & 0xffff0000u) * __uint_as_float(dk[j] & 0xffff0000u));
      o[j] = pk2(bf2f(yv[j] & 0xffffu) + bf2f(sk & 0xffffu), __uint_as_float(yv[j] & 0xffff0000u) + __uint_as_float(sk & 0xffff0000u));
    }
    *(u32x4*)((char*)p.out + (size_t)(idx * 64 + (t >> 3) + 32 * k) * 2048 + 1024 + xcd * 128 + (t & 7) * 16) = o;
  }
  STAMP_AT(1, 1);
  WAIT_VM(0);
  BAR();
  STAMP_AT(1, 2);
  f32x4 acc[4];
#pragma unroll
  for (int c = 0; c < 4; ++c) acc[c] = (f32x4){0, 0, 0, 0};
  const int q4 = e >> 2, pp = e & 3, fw = (2 * g + (q4 >> 1)) & 3;
  LAS char* wbase = sm + C_WO + (4 * g + q4) * 128 + pp * 8;
  const f32x4 zero = {0, 0, 0, 0};
#pragma unroll
  for (int hp = 0; hp < 16; ++hp) {
    const u32x4 kf = *(LAS u32x4*)(sm + C_KV + hp * 1024 + l * 16);
    const bf16x8 zf = *(LAS bf16x8*)(sm + C_ZX + hp * 128 + ((e >> 1) & 1) * 64 + g * 16);
    const f32x4 n0 = mfma16(as4((u32x2){kf[0], kf[1]}), as4((u32x2){pq[hp][0], pq[hp][1]}), zero);
    const f32x4 n1 = mfma16(as4((u32x2){kf[2], kf[3]}), as4((u32x2){pq[hp][2], pq[hp][3]}), zero);
    const f32x4 dn = mfma32(zf, as8(pq[hp]), zero);
    const float r0 = __builtin_amdgcn_rcpf(dn[0] + 1e-6f), r1 = __builtin_amdgcn_rcpf(dn[2] + 1e-6f);
    const u32x4 of = {pk2(n0[0] * r0, n0[1] * r0), pk2(n0[2] * r0, n0[3] * r0), pk2(n1[0] * r1, n1[1] * r1), pk2(n1[2] * r1, n1[3] * r1)};
#pragma unroll
    for (int c = 0; c < 4; ++c) {
      LAS char* wp = wbase + ((c ^ fw) * 32) + hp * 4096;
      acc[c] = mfma32(cat8(trread(wp), trread(wp + 2048)), as8(of), acc[c]);
    }
  }
  STAMP_AT(1, 3);
  LAS char* st = sm + C_ST + w * 2304;
#pragma unroll
  for (int c = 0; c < 4; ++c) *(LAS u32x2*)(st + e * 144 + c * 32 + g * 8) = (u32x2){pk2(acc[c][0], acc[c][1]), pk2(acc[c][2], acc[c][3])};
  __builtin_amdgcn_wave_barrier(); asm volatile("" ::: "memory");
#pragma unroll
  for (int k = 0; k < 2; ++k) {
    const int row = (l >> 3) + 8 * k;
    *(u32x4*)((char*)p.out + (size_t)(rb * 64 + w * 16 + row) * 2048 + cg * 128 + (l & 7) * 16) = *(LAS u32x4*)(st + row * 144 + (l & 7) * 16);
  }
  STAMP_AT(1, 4);
}

extern "C" void kernel_launch(void* const* d_in, const int* in_sizes, int n_in, void* d_out, int out_size, void* d_ws, size_t ws_size,
                              hipStream_t stream) {
  Params p;
  p.x = (const u16*)d_in[0]; p.wqkv = (const u16*)d_in[1]; p.wo = (const u16*)d_in[2]; p.u = (const u16*)d_in[3];
  p.lar = (const u16*)d_in[4]; p.aim = (const u16*)d_in[5]; p.cr = (const u16*)d_in[6]; p.ci = (const u16*)d_in[7];
  p.ldt = (const u16*)d_in[8]; p.dsk = (const u16*)d_in[9]; p.out = (u16*)d_out; p.ws = (char*)d_ws;
  k_one<<<512, 256, 0, stream>>>(p);
  k_two<<<256, 256, 0, stream>>>(p);
}
```

```cpp
#include <hip/hip_runtime.h>
#include <hip/hip_bf16.h>

typedef unsigned short u16;
typedef unsigned int u32;
typedef unsigned long long u64;
typedef short bf16x8 __attribute__((ext_vector_type(8)));
typedef short bf16x4 __attribute__((ext_vector_type(4)));
typedef float f32x4 __attribute__((ext_vector_type(4)));
typedef float f32x2 __attribute__((ext_vector_type(2)));
typedef u32 u32x4 __attribute__((ext_vector_type(4)));
typedef u32 u32x2 __attribute__((ext_vector_type(2)));
typedef __bf16 bfv2 __attribute__((ext_vector_type(2)));
typedef __amdgpu_buffer_rsrc_t rsrc_t;
#define GAS __attribute__((address_space(1)))
#define LAS __attribute__((address_space(3)))
#define DI __device__ __forceinline__
#define RLX_AGENT __ATOMIC_RELAXED, __HIP_MEMORY_SCOPE_AGENT
#define SB() __builtin_amdgcn_sched_barrier(0)

#define STAMP_AT(k, i) do {} while (0)
#define RTDECL do {} while (0)
#define RT(i) do {} while (0)
#define RTDUMP(k) do {} while (0)

DI float bf2f(u32 h) { return __uint_as_float(h << 16); }
DI float bfhi(u32 h) { return __uint_as_float(h & 0xffff0000u); }
DI u32 pk2(float lo, float hi) { bfv2 v = __builtin_convertvector((f32x2){lo, hi}, bfv2); return __builtin_bit_cast(u32, v); }
DI float rbf(float x) { return bf2f(pk2(x, x) & 0xffffu); }
DI f32x4 mfma32(bf16x8 a, bf16x8 b, f32x4 c) { return __builtin_amdgcn_mfma_f32_16x16x32_bf16(a, b, c, 0, 0, 0); }
DI f32x4 mfma16(bf16x4 a, bf16x4 b, f32x4 c) { return __builtin_amdgcn_mfma_f32_16x16x16bf16_1k(a, b, c, 0, 0, 0); }
DI bf16x8 as8(u32x4 v) { return __builtin_bit_cast(bf16x8, v); }
DI bf16x4 as4(u32x2 v) { return __builtin_bit_cast(bf16x4, v); }
DI u32 rfl(u32 v) { return __builtin_amdgcn_readfirstlane(v); }
DI rsrc_t mkrsrc(const void* base, u32 bytes) { return __builtin_amdgcn_make_buffer_rsrc((void*)base, 0, bytes, 0x00020000); }
template <int OFF> DI void dma16(u32 m0v, rsrc_t r, u32 voff, u32 soff) {
  asm volatile("s_mov_b32 m0, %0\n\ts_nop 0\n\tbuffer_load_dwordx4 %1, %2, %3 offen offset:%4 lds"
               :: "s"(m0v), "v"(voff), "s"(r), "s"(soff), "i"(OFF) : "memory");
}
template <int N> DI void wait_vm() { asm volatile("s_waitcnt vmcnt(%0)" :: "i"(N) : "memory"); }
DI void wait_lgkm0() { __builtin_amdgcn_s_waitcnt(0xC07F); asm volatile("" ::: "memory"); }
#define BAR() do { asm volatile("" ::: "memory"); __builtin_amdgcn_s_barrier(); asm volatile("" ::: "memory"); } while (0)
#define LBAR() do { wait_lgkm0(); BAR(); } while (0)
#define WFENCE() do { __builtin_amdgcn_wave_barrier(); asm volatile("" ::: "memory"); } while (0)
DI void st16_dev(rsrc_t r, u32 off, u32x4 v) { __builtin_amdgcn_raw_buffer_store_b128(v, r, off, 0, 16); }
DI u32x4 ld16_dev(rsrc_t r, u32 off) { return __builtin_amdgcn_raw_buffer_load_b128(r, off, 0, 16); }
DI bf16x4 trread(LAS char* a) { return __builtin_amdgcn_ds_read_tr16_b64_v4i16((LAS bf16x4*)a); }
DI bf16x8 cat8(bf16x4 a, bf16x4 b) { return __builtin_shufflevector(a, b, 0, 1, 2, 3, 4, 5, 6, 7); }
DI float phi(float t) { float e = __builtin_amdgcn_exp2f(t * 1.44269504088896f); return (t > 0.f ? t + 1.f : e) + 1e-6f; }
DI f32x2 cmul(f32x2 x, f32x2 w, f32x2 wn) {
  f32x2 t = x * (f32x2){w.x, w.x};
  return __builtin_elementwise_fma((f32x2){x.y, x.x}, wn, t);
}
template <int I> struct IC { static constexpr int v = I; };
template <int B, int E, class F> DI void sfor(F&& f) { if constexpr (B < E) { f(IC<B>{}); sfor<B + 1, E>(f); } }

constexpr size_t WS_EPOCH = 0;
constexpr size_t WS_FIN   = 8192;
constexpr size_t WS_FINZ  = WS_FIN + 16384;
constexpr size_t WS_PART  = 65536;
constexpr size_t WS_PQ    = 2u << 20;
constexpr size_t WS_GU    = 4u << 20;
constexpr size_t WS_YT    = 8u << 20;
constexpr size_t WS_DBG   = 16u << 20;
constexpr int SPIN_CAP = 4096;

struct Params {
  const u16 *x, *wqkv, *wo, *u, *lar, *aim, *cr, *ci, *ldt, *dsk;
  u16* out;
  char* ws;
};
DI u32 sload(const void* p) { return *(const __attribute__((address_space(4))) u32*)p; }

constexpr int A_W = 0, A_X = 49152;
constexpr int ndma(int s) { return (s + 4 <= 15 && s >= -4 ? 3 : 0) + (s >= -5 && s <= 10 ? 1 : 0); }
DI void roleA1(const Params& p, LAS char* sm, int a) {
  const int xcd = a & 7, idx = a >> 3, rbl = idx & 7, cgl = idx >> 3;
  const int rb = (xcd >> 1) * 8 + rbl, cg = (xcd & 1) * 4 + cgl;
  const int w = rfl(threadIdx.x >> 6), l = threadIdx.x & 63, g = l >> 4, e = l & 15;
  const int head = cg * 4 + w;
  const u32 smb = (u32)(size_t)sm;
  RTDECL; RT(0);
  __builtin_amdgcn_s_setprio(2);
  const rsrc_t rx = mkrsrc(p.x, 2048 * 1024), rw = mkrsrc(p.wqkv, 512 * 3072);
  const u32 vx = (u32)((l >> 3) * 1024 + (((l & 7) ^ (l >> 3)) * 16));
  const u32 vw = (u32)((l >> 3) * 3072 + (((l & 7) ^ (((g & 1) | ((w & 1) << 1)) << 1)) * 16));
  const int s0 = (rbl + 2 * cgl) & 7;
  const u32 sxb = (u32)((rb * 64 + w * 8) * 1024), swb = (u32)(w * 8 * 3072 + cg * 128);
  auto dmaW = [&](auto S, auto PT) {
    constexpr int j = S.v + 4, pt = PT.v;
    if constexpr (j >= 0 && j <= 15)
      dma16<pt * 1024>(smb + A_W + (j & 3) * 12288 + w * 3072, rw, vw, swb + (u32)((((s0 + (j >> 1)) & 7) * 2 + (j & 1)) * (32 * 3072)));
  };
  auto dmaX = [&](auto S) {
    constexpr int s = S.v, m = (s + 5) >> 1, hf = (s + 5) & 1;
    if constexpr (s >= -5 && s <= 10)
      dma16<0>(smb + A_X + (m % 3) * 8192 + (hf * 4 + w) * 1024, rx, vx, sxb + (u32)(hf * 32 * 1024 + ((s0 + m) & 7) * 128));
  };
  sfor<-5, 0>([&](auto S) { dmaX(S); dmaW(S, IC<0>{}); dmaW(S, IC<1>{}); dmaW(S, IC<2>{}); });
  const u32 tag = sload(p.ws + WS_EPOCH + cg * 256) + 1u;
  const int xo = e * 128 + ((g ^ (l & 7)) * 16);
  const int q4 = e >> 2, pp = e & 3;
  const int wo_ = g * 3072 + q4 * 128 + ((w ^ (((q4 >> 1) & 1) | ((g & 1) << 1))) * 32) + pp * 8;
  LAS char* xp0 = sm + A_X + xo; LAS char* xp1 = sm + A_X + (xo ^ 64); LAS char* wp = sm + A_W + wo_;
  f32x4 aq[4], ak[4], av[4];
#pragma unroll
  for (int t = 0; t < 4; ++t) { aq[t] = (f32x4){0, 0, 0, 0}; ak[t] = aq[t]; av[t] = aq[t]; }
  bf16x8 xf[2][4], wf[2][3];
  auto rdX = [&](auto J, auto T) { constexpr int j = J.v, t = T.v;
    xf[j & 1][t] = *(LAS bf16x8*)(((j & 1) ? xp1 : xp0) + ((j >> 1) % 3) * 8192 + t * 2048); };
  auto rdW = [&](auto J, auto PT) { constexpr int j = J.v, pt = PT.v;
    wf[j & 1][pt] = cat8(trread(wp + (j & 3) * 12288 + pt * 1024), trread(wp + (j & 3) * 12288 + pt * 1024 + 512)); };
  wait_vm<ndma(-3) + ndma(-2) + ndma(-1)>();
  BAR();
  RT(5);
  sfor<0, 3>([&](auto PT) { rdW(IC<0>{}, PT); });
  sfor<0, 4>([&](auto T) { rdX(IC<0>{}, T); });
  sfor<0, 16>([&](auto J) {
    constexpr int j = J.v, c = j & 1;
    auto mm = [&](auto T) { constexpr int t = T.v;
      ak[t] = mfma32(xf[c][t], wf[c][1], ak[t]); av[t] = mfma32(xf[c][t], wf[c][2], av[t]); aq[t] = mfma32(wf[c][0], xf[c][t], aq[t]); };
    if constexpr (j < 15) {
      wait_vm<ndma(j - 2) + ndma(j - 1)>();
      wait_lgkm0();
      BAR();
      SB();
      rdW(IC<j + 1>{}, IC<1>{}); rdX(IC<j + 1>{}, IC<0>{}); rdW(IC<j + 1>{}, IC<2>{});
      dmaW(J, IC<0>{});
      SB();
      mm(IC<0>{});
      SB();
      rdW(IC<j + 1>{}, IC<0>{}); rdX(IC<j + 1>{}, IC<1>{});
      dmaW(J, IC<1>{});
      SB();
      mm(IC<1>{});
      SB();
      rdX(IC<j + 1>{}, IC<2>{}); rdX(IC<j + 1>{}, IC<3>{});
      dmaW(J, IC<2>{});
      SB();
      mm(IC<2>{});
      SB();
      dmaX(J);
      SB();
      mm(IC<3>{});
      SB();
    } else {
      sfor<0, 4>(mm);
    }
  });
  RT(1);
  f32x4 kv = {0, 0, 0, 0}, zz = {0, 0, 0, 0};
  const u32x4 ones = {0x3f803f80u, 0x3f803f80u, 0x3f803f80u, 0x3f803f80u};
#pragma unroll
  for (int h = 0; h < 2; ++h) {
    u32x4 kf = {pk2(phi(ak[2 * h][0]), phi(ak[2 * h][1])), pk2(phi(ak[2 * h][2]), phi(ak[2 * h][3])),
                pk2(phi(ak[2 * h + 1][0]), phi(ak[2 * h + 1][1])), pk2(phi(ak[2 * h + 1][2]), phi(ak[2 * h + 1][3]))};
    u32x4 vf = {pk2(av[2 * h][0], av[2 * h][1]), pk2(av[2 * h][2], av[2 * h][3]),
                pk2(av[2 * h + 1][0], av[2 * h + 1][1]), pk2(av[2 * h + 1][2], av[2 * h + 1][3])};
    kv = mfma32(as8(kf), as8(vf), kv);
    zz = mfma32(as8(kf), as8(ones), zz);
  }
  const rsrc_t rpart = mkrsrc(p.ws + WS_PART, 32 * 9 * 4096);
  st16_dev(rpart, (u32)(((head * 9 + (l >> 3)) * 32 + rb) * 128 + (l & 7) * 16), (u32x4){pk2(kv[0], kv[1]), tag, pk2(kv[2], kv[3]), tag});
  if (e < 2) st16_dev(rpart, (u32)(((head * 9 + 8) * 32 + rb) * 128 + (e * 4 + g) * 16), (u32x4){pk2(zz[0], zz[1]), tag, pk2(zz[2], zz[3]), tag});
  SB();
  RT(2);
  {
    char* pq = p.ws + WS_PQ + ((size_t)(rb * 2) * 32 + head) * 1024 + l * 16;
#pragma unroll
    for (int h = 0; h < 2; ++h)
      *(u32x4*)(pq + h * 32768) = (u32x4){pk2(phi(aq[2 * h][0]), phi(aq[2 * h][1])), pk2(phi(aq[2 * h][2]), phi(aq[2 * h][3])),
                                          pk2(phi(aq[2 * h + 1][0]), phi(aq[2 * h + 1][1])), pk2(phi(aq[2 * h + 1][2]), phi(aq[2 * h + 1][3]))};
  }
  RT(3);
  if (rb < 9) {
    const u32 off = (u32)((head * 9 + rb) * 4096 + l * 16);
    u32x4 v[4];
#pragma unroll 1
    for (int spin = 0; spin < SPIN_CAP; ++spin) {
      bool ok = true;
      asm volatile("" ::: "memory");
#pragma unroll
      for (int i = 0; i < 4; ++i) { v[i] = ld16_dev(rpart, off + i * 1024); ok &= (v[i][1] == tag) & (v[i][3] == tag); }
      if (__all(ok)) break;
      __builtin_amdgcn_s_sleep(1);
    }
    float s[4] = {0.f, 0.f, 0.f, 0.f};
#pragma unroll
    for (int i = 0; i < 4; ++i) { s[0] += bf2f(v[i][0]); s[1] += bfhi(v[i][0]); s[2] += bf2f(v[i][2]); s[3] += bfhi(v[i][2]); }
#pragma unroll
    for (int j = 0; j < 4; ++j) { s[j] += __shfl_xor(s[j], 8); s[j] += __shfl_xor(s[j], 16); s[j] += __shfl_xor(s[j], 32); }
    const u32x2 r = {pk2(s[0], s[1]), pk2(s[2], s[3])};
    if (rb < 8) { if (l < 8) *(u32x2*)(p.ws + WS_FIN + (head >> 1) * 1024 + (rb * 8 + l) * 16 + (head & 1) * 8) = r; }
    else if (l < 4) *(u32x2*)(p.ws + WS_FINZ + head * 32 + l * 8) = r;
    if (rb == 8) { BAR(); if (threadIdx.x == 0) __hip_atomic_store((GAS u32*)(p.ws + WS_EPOCH + cg * 256), tag, RLX_AGENT); }
  }
  RT(4);
  RTDUMP(0);
}

constexpr int B_SCR = 0;
constexpr int B_UT = 37120;
constexpr int B_UC = 46336;
constexpr int UC_S = 4160;
constexpr int B_KA = 62976;
constexpr int B_PX = 72192;
static_assert(B_PX + 8192 <= 81920, "lds");
DI void roleB1(const Params& p, LAS char* sm, int b) {
  const int gch = b & 7, bi = b >> 3;
  const int t = threadIdx.x, w = rfl(t >> 6), l = t & 63, g = l >> 4, e = l & 15;
  const int chl = w >> 1, mh = w & 1, ch = gch * 64 + bi * 2 + chl;
  RTDECL; RT(0);
  const char* ut = (const char*)p.u + (size_t)(bi * 64 + (t >> 3)) * 1024 + gch * 128 + (t & 7) * 16;
  const u32x4 u0 = *(const u32x4*)ut, u1 = *(const u32x4*)(ut + 32 * 1024);
  const int pi = ch * 128 + mh * 64 + l;
  const u32 r_lar = p.lar[pi], r_aim = p.aim[pi], r_cr = p.cr[pi], r_ci = p.ci[pi], r_ldt = p.ldt[ch];
  const u32 tag = sload(p.ws + WS_EPOCH + (8 + gch) * 256) + 1u;
  if (t < 64) *(LAS u32x4*)(sm + B_KA + (t >> 5) * 4608 + (t & 31) * 16) = (u32x4){0, 0, 0, 0};
  else if (t < 96) *(LAS u32*)(sm + B_UC + ((t - 64) >> 3) * UC_S + (t & 7) * 4) = 0u;
  *(LAS u32x4*)(sm + B_UT + (t >> 3) * 144 + (t & 7) * 16) = u0;
  *(LAS u32x4*)(sm + B_UT + ((t >> 3) + 32) * 144 + (t & 7) * 16) = u1;
  asm volatile("" :: "s"(tag) : "memory");
  LBAR();
  const rsrc_t rgu = mkrsrc(p.ws + WS_GU, 512 * 8192);
  {
    const int cq = w * 4 + g;
    u32x2 r[4];
#pragma unroll
    for (int j = 0; j < 4; ++j) r[j] = *(LAS u32x2*)(sm + B_UT + (e * 4 + j) * 144 + cq * 8);
    const u32 off = (u32)((gch * 64 + cq * 4) * 8192 + (bi * 16 + e) * 16);
#pragma unroll
    for (int c = 0; c < 4; ++c) {
      const u32 sel = (c & 1) ? 0x07060302u : 0x05040100u;
      const u32 d0 = __builtin_amdgcn_perm(r[1][c >> 1], r[0][c >> 1], sel), d1 = __builtin_amdgcn_perm(r[3][c >> 1], r[2][c >> 1], sel);
      st16_dev(rgu, off + c * 8192, (u32x4){d0, tag, d1, tag});
    }
  }
  SB();
  RT(1);
  const float dt = rbf(expf(bf2f(r_ldt)));
  const float a2 = -rbf(expf(bf2f(r_lar))) * dt * 1.44269504088896f;
  const float th = bf2f(r_aim) * dt * 0.15915494309189535f;
  auto wpow = [&](float s) -> f32x2 {
    const float m = __builtin_amdgcn_exp2f(a2 * s), f = __builtin_amdgcn_fractf(th * s);
    return (f32x2){m * __builtin_amdgcn_cosf(f), m * __builtin_amdgcn_sinf(f)};
  };
  const f32x2 w1 = wpow(1.f), w8 = wpow(8.f), w128 = wpow(128.f);
  const f32x2 w1n = {-w1.y, w1.y}, w128n = {-w128.y, w128.y}, w8cn = {w8.y, -w8.y};
  LAS char* scr = sm + B_SCR + w * 9248;
  LAS u32* sw = (LAS u32*)(scr + l * 4);
  const int fo = e * 272 + g * 16;
  auto rdf = [&](int reg, int sh, int ks) { return *(LAS bf16x8*)(scr + reg * 4624 + fo + sh * 272 + ks * 64); };
  u32 pc[17], pr[16];
  f32x2 G[8];
  {
    f32x2 c = {1.f, 0.f};
    pc[0] = 0x3f80u;
    G[0] = (f32x2){2.f * bf2f(r_cr) * dt, 2.f * bf2f(r_ci) * dt};
    pr[0] = pk2(G[0].x, G[0].y);
#pragma unroll
    for (int i = 1; i <= 16; ++i) {
      c = cmul(c, w8, w8cn); pc[i] = pk2(c.x, c.y);
      if (i < 8) { G[i] = cmul(G[i - 1], w1, w1n); pr[i] = pk2(G[i].x, G[i].y); }
      else if (i < 16) { G[i - 8] = cmul(G[i - 8], w128, w128n); pr[i] = pk2(G[i - 8].x, G[i - 8].y); }
    }
  }
  SB();
#pragma unroll
  for (int i = 0; i <= 16; ++i) sw[i * 68] = pc[i];
#pragma unroll
  for (int i = 0; i < 16; ++i) sw[1156 + i * 68] = pr[i];
  WFENCE();
  bf16x8 cf[4], cf2[4], af[4];
#pragma unroll
  for (int ks = 0; ks < 4; ++ks) { cf[ks] = rdf(0, 0, ks); af[ks] = rdf(1, 0, ks); cf2[ks] = rdf(0, 1, ks); }
  WFENCE();
  SB();
  u32 ps[16], pt_[8];
#pragma unroll
  for (int h = 0; h < 2; ++h)
#pragma unroll
    for (int i = 0; i < 4; ++i) { G[2 * i] = cmul(G[2 * i], w128, w128n); ps[4 * h + i] = pk2(G[2 * i].x, G[2 * i].y); }
#pragma unroll
  for (int h = 0; h < 4; ++h)
#pragma unroll
    for (int i = 0; i < 2; ++i) { G[4 * i] = cmul(G[4 * i], w128, w128n); ps[8 + 2 * h + i] = pk2(G[4 * i].x, G[4 * i].y); }
#pragma unroll
  for (int h = 0; h < 8; ++h) { G[0] = cmul(G[0], w128, w128n); pt_[h] = pk2(G[0].x, G[0].y); }
  SB();
#pragma unroll
  for (int i = 0; i < 16; ++i) sw[i * 68] = ps[i];
#pragma unroll
  for (int h = 0; h < 8; ++h) sw[1156 + (4 * (h >> 1) + (h & 1)) * 68] = pt_[h];
  WFENCE();
  bf16x8 a1[4], a2f[4];
#pragma unroll
  for (int ks = 0; ks < 4; ++ks) { a1[ks] = rdf(0, 0, ks); a2f[ks] = rdf(1, 0, ks); }
  const u32 uoff = (u32)(ch * 8192 + (mh * 256 + l) * 16);
  u32x4 v[4], vp;
#pragma unroll
  for (int i = 0; i < 4; ++i) v[i] = ld16_dev(rgu, uoff + i * 1024);
  vp = ld16_dev(rgu, (u32)(ch * 8192 + 255 * 16));
  SB();
  f32x4 d0 = {0, 0, 0, 0}, d1 = d0, d1s = d0, d2 = d0, d2s = d0;
#pragma unroll
  for (int ks = 0; ks < 4; ++ks) d0 = mfma32(af[ks], cf[ks], d0);
#pragma unroll
  for (int ks = 0; ks < 4; ++ks) {
    d1 = mfma32(a1[ks], cf[ks], d1); d1s = mfma32(a1[ks], cf2[ks], d1s);
    d2 = mfma32(a2f[ks], cf[ks], d2); d2s = mfma32(a2f[ks], cf2[ks], d2s);
  }
  {
    const f32x4 pa = {d2[0], d2[1], d2s[0], d2s[1]}, pb = {d1s[0], d1s[2], 0.f, 0.f};
    LAS f32x4* px = (LAS f32x4*)(sm + B_PX + w * 2048 + l * 16);
    px[0] = mh ? d0 : d1; px[64] = mh ? pa : pb;
    RT(2);
    LBAR();
    LAS f32x4* py = (LAS f32x4*)(sm + B_PX + (w ^ 1) * 2048 + l * 16);
    const f32x4 qa = py[0], qb = py[64];
    LAS char* ka = sm + B_KA + chl * 4608 + 512 + e * 16;
    if (mh == 0) {
      const f32x4 k0 = d0 + qa, k2 = pa + qb;
      *(LAS u32x2*)(ka + (g >> 1) * 256 + ((g & 1) ^ 1) * 8) = (u32x2){pk2(k0[3], k0[2]), pk2(k0[1], k0[0])};
#pragma unroll
      for (int r = 0; r < 2; ++r) {
        const float v_ = k2[r], w_ = k2[2 + r], m_ = (e & 1) ? w_ : v_;
        const u32 vv = pk2(v_, v_);
        *(LAS u32x4*)(ka + 2048 + (2 * g + r) * 256) = (u32x4){pk2(w_, w_), pk2(w_, m_), vv, vv};
      }
    } else {
      const f32x4 k1 = d1 + qa, k1s = pb + qb;
      const bool lo = g < 2;
      const float x_ = lo ? k1[2] : k1[1];
      *(LAS u32x4*)(ka + (lo ? 512 + g * 256 : 1024 + (g - 2) * 512)) =
          (u32x4){pk2(k1s[0], lo ? k1[3] : k1s[0]), pk2(x_, x_), pk2(x_, lo ? k1[1] : k1[0]), pk2(k1[0], k1[0])};
      if (!lo) *(LAS u32x4*)(ka + 1024 + (g - 2) * 512 + 256) = (u32x4){pk2(k1s[1], k1s[1]), pk2(k1[3], k1[3]), pk2(k1[3], k1[2]), pk2(k1[2], k1[2])};
    }
  }
#pragma unroll 1
  for (int spin = 0; spin < SPIN_CAP; ++spin) {
    bool ok = vp[3] == tag;
#pragma unroll
    for (int i = 0; i < 4; ++i) ok &= (v[i][1] == tag) & (v[i][3] == tag);
    if (__all(ok)) break;
    __builtin_amdgcn_s_sleep(1);
    asm volatile("" ::: "memory");
#pragma unroll
    for (int i = 0; i < 4; ++i) v[i] = ld16_dev(rgu, uoff + i * 1024);
    vp = ld16_dev(rgu, (u32)(ch * 8192 + 255 * 16));
  }
  RT(3);
  {
    LAS char* uc = sm + B_UC + chl * 2 * UC_S + 4 + 28 + (mh * 256 + l) * 8;
    u32 carry = mh ? vp[2] : 0u;
#pragma unroll
    for (int i = 0; i < 4; ++i) {
      const u32 prev = __builtin_amdgcn_update_dpp(carry, v[i][2], 0x138  , 0xf, 0xf, false);
      carry = __builtin_amdgcn_readlane(v[i][2], 63);
      *(LAS u32x2*)(uc + UC_S + i * 512) = (u32x2){v[i][0], v[i][2]};
      *(LAS u32x2*)(uc + i * 512) = (u32x2){__builtin_amdgcn_alignbit(v[i][0], prev, 16), __builtin_amdgcn_alignbit(v[i][2], v[i][0], 16)};
    }
  }
  LBAR();
  if (bi == 0 && t == 0) __hip_atomic_store((GAS u32*)(p.ws + WS_EPOCH + (8 + gch) * 256), tag, RLX_AGENT);
  RT(4);
  f32x4 acc[8];
#pragma unroll
  for (int c = 0; c < 8; ++c) acc[c] = (f32x4){0, 0, 0, 0};
  {
    LAS char* tb = sm + B_UC + (chl * 2 + (e & 1)) * UC_S + 4 + (16 * (g >> 1) - 8 * (g & 1) + 8 + (e & ~1)) * 2 + mh * 256;
    LAS char* vb = sm + B_KA + chl * 4608 + 512 + (e - (g >> 1)) * 32 + (g & 1) * 16 - mh * 256 - 192;
    bf16x8 T[2][8], V[2][8];
    auto rdT = [&](int ee, int m) { LAS u32* tp = (LAS u32*)(tb + ee * 64 + m * 512); T[ee & 1][m] = as8((u32x4){tp[0], tp[1], tp[2], tp[3]}); };
    auto rdV = [&](int ee, int m) { V[ee & 1][m] = *(LAS bf16x8*)(vb + (3 - ee) * 64 + m * 512); };
#pragma unroll
    for (int m = 0; m < 8; ++m) { rdT(0, m); rdV(0, m); }
    SB();
#pragma unroll
    for (int ee = 0; ee < 4; ++ee) {
#pragma unroll
      for (int s = 0; s < 8; ++s) {
        if (ee < 3) { rdT(ee + 1, s); rdV(ee + 1, s); }
#pragma unroll
        for (int m = 0; m <= s; ++m) {
          if (m + s < 8) acc[m + s] = mfma32(V[ee & 1][s], T[ee & 1][m], acc[m + s]);
          if (m < s && m + s < 8) acc[m + s] = mfma32(V[ee & 1][m], T[ee & 1][s], acc[m + s]);
        }
        SB();
      }
    }
  }
  RT(5);
#pragma unroll
  for (int c = 0; c < 8; ++c) *(LAS f32x4*)(sm + B_SCR + (w * 8 + c) * 1024 + l * 16) = acc[c];
  LBAR();
#pragma unroll
  for (int k = 0; k < 2; ++k) {
    const int c = 2 * w + k;
    LAS f32x4* xp = (LAS f32x4*)(sm + B_SCR + c * 1024 + l * 16);
    const f32x4 y0 = xp[0] + xp[512], y1 = xp[1024] + xp[1536];
    *(u32x4*)(p.ws + WS_YT + (size_t)(((gch * 32 + 4 * c + g) * 32 + bi) * 256 + e * 16)) =
        (u32x4){pk2(y0[0], y1[0]), pk2(y0[1], y1[1]), pk2(y0[2], y1[2]), pk2(y0[3], y1[3])};
  }
  RT(6);
  RTDUMP(0);
}

__global__ __launch_bounds__(256, 2) void k_one(Params p) {
  __shared__ __attribute__((aligned(16))) char smem[81920];
  if (blockIdx.x < 256) roleB1(p, (LAS char*)smem, blockIdx.x); else roleA1(p, (LAS char*)smem, blockIdx.x - 256);
}

constexpr int C_WO = 0, C_KV = 65536, C_ZX = 81920, C_YT = 86016, C_XC = 95232, C_ST = 111616, C_END = C_ST + 4 * 2304;
__global__ __launch_bounds__(256, 2) void k_two(Params p) {
  __shared__ __attribute__((aligned(16))) char smem[C_END];
  LAS char* sm = (LAS char*)smem;
  const int a = blockIdx.x, xcd = a & 7, idx = a >> 3;
  const int rb = (xcd >> 1) * 8 + (idx & 7), cg = (xcd & 1) * 4 + (idx >> 3);
  const int t = threadIdx.x, w = rfl(t >> 6), l = t & 63, g = l >> 4, e = l & 15;
  const int sh = w & 1, kh = w >> 1;
  const u32 smb = (u32)(size_t)sm;
  RTDECL; RT(0);
  const rsrc_t rfin = mkrsrc(p.ws + WS_FIN, 16384), rwo = mkrsrc(p.wo, 512 * 1024);
  sfor<0, 4>([&](auto I) { dma16<I.v * 1024>(smb + C_KV + w * 4096, rfin, (u32)(l * 16), (u32)(w * 4096)); });
  const u32x2 zr = *(const u32x2*)(p.ws + WS_FINZ + (t >> 3) * 32 + (t & 3) * 8);
  u32x4 pq[16];
  {
    const char* pp_ = p.ws + WS_PQ + (size_t)((rb * 2 + sh) * 32 + kh * 16) * 1024 + l * 16;
#pragma unroll
    for (int i = 0; i < 16; ++i) pq[i] = *(const u32x4*)(pp_ + i * 1024);
  }
  const char* yt = p.ws + WS_YT + (size_t)(xcd * 32 + idx) * 8192 + t * 16;
  const u32x4 y0 = *(const u32x4*)yt, y1 = *(const u32x4*)(yt + 4096);
  const char* ut = (const char*)p.u + (size_t)(idx * 64 + (t >> 3)) * 1024 + xcd * 128 + (t & 7) * 16;
  const u32x4 u0 = *(const u32x4*)ut, u1 = *(const u32x4*)(ut + 32 * 1024);
  const u32x4 dk = *(const u32x4*)((const char*)p.dsk + xcd * 128 + (t & 7) * 16);
  SB();
  {
    const u32 vw = (u32)((l >> 3) * 1024 + (((l & 7) ^ (g << 1)) * 16));
    sfor<0, 16>([&](auto I) { constexpr int hp = (I.v >> 1) + (I.v & 1) * 8;
      dma16<0>(smb + C_WO + (hp * 4 + w) * 1024, rwo, vw, (u32)(cg * 128 + (hp * 32 + w * 8) * 1024)); });
  }
  SB();
  wait_vm<16>();
  asm volatile("" : "+v"(*(u32x2*)&zr));
  *(LAS u32x4*)(sm + C_ZX + t * 16) = (t & 4) ? (u32x4){0, 0, zr[0], zr[1]} : (u32x4){zr[0], zr[1], 0, 0};
#pragma unroll
  for (int r = 0; r < 4; ++r) {
    *(LAS u32*)(sm + C_YT + (16 * r + e) * 144 + (t >> 4) * 4) = y0[r];
    *(LAS u32*)(sm + C_YT + (16 * r + e) * 144 + ((t >> 4) + 16) * 4) = y1[r];
  }
  LBAR();
  RT(1);
  const f32x4 zero = {0, 0, 0, 0};
  u32x4 of[2][8];
#pragma unroll
  for (int j = 0; j < 8; ++j) {
    const u32x4 kf = *(LAS u32x4*)(sm + C_KV + (kh * 8 + j) * 1024 + l * 16);
#pragma unroll
    for (int hh = 0; hh < 2; ++hh) {
      const u32x4 U = pq[2 * j + hh];
      const bf16x8 zf = *(LAS bf16x8*)(sm + C_ZX + (kh * 16 + 2 * j + hh) * 128 + ((e >> 1) & 1) * 64 + g * 16);
      const bf16x4 kh4 = as4((u32x2){kf[2 * hh], kf[2 * hh + 1]});
      const f32x4 na = mfma16(kh4, as4((u32x2){U[0], U[1]}), zero);
      const f32x4 nb = mfma16(kh4, as4((u32x2){U[2], U[3]}), zero);
      const f32x4 dn = mfma32(zf, as8(U), zero);
      const float ra = __builtin_amdgcn_rcpf(dn[0] + 1e-6f), rb_ = __builtin_amdgcn_rcpf(dn[2] + 1e-6f);
      of[0][j][2 * hh] = pk2(na[0] * ra, na[1] * ra); of[0][j][2 * hh + 1] = pk2(na[2] * ra, na[3] * ra);
      of[1][j][2 * hh] = pk2(nb[0] * rb_, nb[1] * rb_); of[1][j][2 * hh + 1] = pk2(nb[2] * rb_, nb[3] * rb_);
    }
  }
#pragma unroll
  for (int k = 0; k < 2; ++k) {
    const u32x4 yv = *(LAS u32x4*)(sm + C_YT + ((t >> 3) + 32 * k) * 144 + (t & 7) * 16);
    const u32x4 uv = k ? u1 : u0;
    u32x4 o;
#pragma unroll
    for (int j = 0; j < 4; ++j) {
      const u32 sk = pk2(bf2f(uv[j]) * bf2f(dk[j]), bfhi(uv[j]) * bfhi(dk[j]));
      o[j] = pk2(bf2f(yv[j]) + bf2f(sk), bfhi(yv[j]) + bfhi(sk));
    }
    *(u32x4*)((char*)p.out + (size_t)(idx * 64 + (t >> 3) + 32 * k) * 2048 + 1024 + xcd * 128 + (t & 7) * 16) = o;
  }
  SB();
  RT(2);
  wait_vm<2>();
  BAR();
  RT(3);
  f32x4 acc[2][4];
#pragma unroll
  for (int c = 0; c < 4; ++c) { acc[0][c] = zero; acc[1][c] = zero; }
  const int q4 = e >> 2, pp = e & 3, fw = (2 * g + (q4 >> 1)) & 3;
  LAS char* wbase = sm + C_WO + kh * 32768 + (4 * g + q4) * 128 + pp * 8;
  bf16x8 wf[2][4];
  auto rdw = [&](int j, int c) { LAS char* wq = wbase + ((c ^ fw) * 32) + j * 4096; wf[j & 1][c] = cat8(trread(wq), trread(wq + 2048)); };
#pragma unroll
  for (int c = 0; c < 4; ++c) rdw(0, c);
  SB();
#pragma unroll
  for (int j = 0; j < 8; ++j) {
#pragma unroll
    for (int c = 0; c < 4; ++c) {
      if (j < 7) rdw(j + 1, c);
      acc[0][c] = mfma32(wf[j & 1][c], as8(of[0][j]), acc[0][c]);
      acc[1][c] = mfma32(wf[j & 1][c], as8(of[1][j]), acc[1][c]);
      SB();
    }
  }
  RT(4);
  f32x4 mine[4];
#pragma unroll
  for (int c = 0; c < 4; ++c) {
    *(LAS f32x4*)(sm + C_XC + (w * 4 + c) * 1024 + l * 16) = kh ? acc[0][c] : acc[1][c];
    mine[c] = kh ? acc[1][c] : acc[0][c];
  }
  LBAR();
  LAS char* st = sm + C_ST + w * 2304;
#pragma unroll
  for (int c = 0; c < 4; ++c) {
    const f32x4 s = mine[c] + *(LAS f32x4*)(sm + C_XC + ((w ^ 2) * 4 + c) * 1024 + l * 16);
    *(LAS u32x2*)(st + e * 144 + c * 32 + g * 8) = (u32x2){pk2(s[0], s[1]), pk2(s[2], s[3])};
  }
  WFENCE();
#pragma unroll
  for (int k = 0; k < 2; ++k) {
    const int row = (l >> 3) + 8 * k;
    *(u32x4*)((char*)p.out + (size_t)(rb * 64 + (sh * 2 + kh) * 16 + row) * 2048 + cg * 128 + (l & 7) * 16) = *(LAS u32x4*)(st + row * 144 + (l & 7) * 16);
  }
  RT(5);
  RTDUMP(1);
}

extern "C" void kernel_launch(void* const* d_in, const int* in_sizes, int n_in, void* d_out, int out_size, void* d_ws, size_t ws_size,
                              hipStream_t stream) {
  Params p;
  p.x = (const u16*)d_in[0]; p.wqkv = (const u16*)d_in[1]; p.wo = (const u16*)d_in[2]; p.u = (const u16*)d_in[3];
  p.lar = (const u16*)d_in[4]; p.aim = (const u16*)d_in[5]; p.cr = (const u16*)d_in[6]; p.ci = (const u16*)d_in[7];
  p.ldt = (const u16*)d_in[8]; p.dsk = (const u16*)d_in[9]; p.out = (u16*)d_out; p.ws = (char*)d_ws;
  k_one<<<512, 256, 0, stream>>>(p);
  k_two<<<256, 256, 0, stream>>>(p);
}
```

```cpp
#define STAMP
#define DIAG
#define DIAGW 4
#define D1K 0
#define D1H 1
#define D1S 0
#define D1M 2
#define D2K 0
#define D2H 1
#define D2S 5
#define D2M 1
#include <hip/hip_runtime.h>
#include <hip/hip_bf16.h>

typedef unsigned short u16;
typedef unsigned int u32;
typedef unsigned long long u64;
typedef short bf16x8 __attribute__((ext_vector_type(8)));
typedef short bf16x4 __attribute__((ext_vector_type(4)));
typedef float f32x4 __attribute__((ext_vector_type(4)));
typedef float f32x2 __attribute__((ext_vector_type(2)));
typedef u32 u32x4 __attribute__((ext_vector_type(4)));
typedef u32 u32x2 __attribute__((ext_vector_type(2)));
typedef __bf16 bfv2 __attribute__((ext_vector_type(2)));
typedef __amdgpu_buffer_rsrc_t rsrc_t;
#define GAS __attribute__((address_space(1)))
#define LAS __attribute__((address_space(3)))
#define DI __device__ __forceinline__
#define RLX_AGENT __ATOMIC_RELAXED, __HIP_MEMORY_SCOPE_AGENT
#define SB() __builtin_amdgcn_sched_barrier(0)

#define STAMP_AT(k, i) do { if ((threadIdx.x & 63) == 0) ((u64*)(p.ws + WS_DBG))[(((k) * 512 + blockIdx.x) * 4 + (threadIdx.x >> 6)) * 16 + (i)] = __builtin_amdgcn_s_memrealtime(); } while (0)
#define RTDECL u64 rtk[12] = {0,0,0,0,0,0,0,0,0,0,0,0}, ctk[12] = {0,0,0,0,0,0,0,0,0,0,0,0}
#define RT(i) do { SB(); asm volatile("s_memrealtime %0\n\ts_memtime %1\n\ts_waitcnt lgkmcnt(0)" : "=s"(rtk[i]), "=s"(ctk[i]) :: "memory"); SB(); } while (0)
#define RTDUMP(k) do { if ((threadIdx.x & 63) == 0) for (int i_ = 0; i_ < 12; ++i_) { ((u64*)(p.ws + WS_DBG))[(((k) * 512 + blockIdx.x) * 8 + (threadIdx.x >> 6)) * 16 + i_] = rtk[i_]; ((u64*)(p.ws + WS_DBG + (4u << 20)))[(((k) * 512 + blockIdx.x) * 8 + (threadIdx.x >> 6)) * 16 + i_] = ctk[i_]; } } while (0)

DI float bf2f(u32 h) { return __uint_as_float(h << 16); }
DI float bfhi(u32 h) { return __uint_as_float(h & 0xffff0000u); }
DI u32 pk2(float lo, float hi) { bfv2 v = __builtin_convertvector((f32x2){lo, hi}, bfv2); return __builtin_bit_cast(u32, v); }
DI float rbf(float x) { return bf2f(pk2(x, x) & 0xffffu); }
DI f32x4 mfma32(bf16x8 a, bf16x8 b, f32x4 c) { return __builtin_amdgcn_mfma_f32_16x16x32_bf16(a, b, c, 0, 0, 0); }
DI f32x4 mfma16(bf16x4 a, bf16x4 b, f32x4 c) { return __builtin_amdgcn_mfma_f32_16x16x16bf16_1k(a, b, c, 0, 0, 0); }
DI bf16x8 as8(u32x4 v) { return __builtin_bit_cast(bf16x8, v); }
DI bf16x4 as4(u32x2 v) { return __builtin_bit_cast(bf16x4, v); }
DI u32 rfl(u32 v) { return __builtin_amdgcn_readfirstlane(v); }
DI rsrc_t mkrsrc(const void* base, u32 bytes) { return __builtin_amdgcn_make_buffer_rsrc((void*)base, 0, bytes, 0x00020000); }
template <int OFF> DI void dma16(u32 m0v, rsrc_t r, u32 voff, u32 soff) {
  asm volatile("s_mov_b32 m0, %0\n\ts_nop 0\n\tbuffer_load_dwordx4 %1, %2, %3 offen offset:%4 lds"
               :: "s"(m0v), "v"(voff), "s"(r), "s"(soff), "i"(OFF) : "memory");
}
template <int N> DI void wait_vm() { asm volatile("s_waitcnt vmcnt(%0)" :: "i"(N) : "memory"); }
DI void wait_lgkm0() { __builtin_amdgcn_s_waitcnt(0xC07F); asm volatile("" ::: "memory"); }
#define BAR() do { asm volatile("" ::: "memory"); __builtin_amdgcn_s_barrier(); asm volatile("" ::: "memory"); } while (0)
#define LBAR() do { wait_lgkm0(); BAR(); } while (0)
#define WFENCE() do { __builtin_amdgcn_wave_barrier(); asm volatile("" ::: "memory"); } while (0)
DI void st16_dev(rsrc_t r, u32 off, u32x4 v) { __builtin_amdgcn_raw_buffer_store_b128(v, r, off, 0, 16); }
DI u32x4 ld16_dev(rsrc_t r, u32 off) { return __builtin_amdgcn_raw_buffer_load_b128(r, off, 0, 16); }
DI bf16x4 trread(LAS char* a) { return __builtin_amdgcn_ds_read_tr16_b64_v4i16((LAS bf16x4*)a); }
DI bf16x8 cat8(bf16x4 a, bf16x4 b) { return __builtin_shufflevector(a, b, 0, 1, 2, 3, 4, 5, 6, 7); }
DI float phi(float t) { float e = __builtin_amdgcn_exp2f(t * 1.44269504088896f); return (t > 0.f ? t + 1.f : e) + 1e-6f; }
DI f32x2 cmul(f32x2 x, f32x2 w, f32x2 wn) {
  f32x2 t = x * (f32x2){w.x, w.x};
  return __builtin_elementwise_fma((f32x2){x.y, x.x}, wn, t);
}
template <int I> struct IC { static constexpr int v = I; };
template <int B, int E, class F> DI void sfor(F&& f) { if constexpr (B < E) { f(IC<B>{}); sfor<B + 1, E>(f); } }

constexpr size_t WS_EPOCH = 0;
constexpr size_t WS_FIN   = 8192;
constexpr size_t WS_FINZ  = WS_FIN + 16384;
constexpr size_t WS_PART  = 65536;
constexpr size_t WS_PQ    = 2u << 20;
constexpr size_t WS_GU    = 4u << 20;
constexpr size_t WS_YT    = 8u << 20;
constexpr size_t WS_DBG   = 16u << 20;
constexpr int SPIN_CAP = 4096;

struct Params {
  const u16 *x, *wqkv, *wo, *u, *lar, *aim, *cr, *ci, *ldt, *dsk;
  u16* out;
  char* ws;
};
DI u32 sload(const void* p) { return *(const __attribute__((address_space(4))) u32*)p; }

constexpr int A_W = 0, A_X = 49152;
constexpr int ndma(int s) { return (s + 4 <= 15 && s >= -4 ? 3 : 0) + (s >= -5 && s <= 10 ? 1 : 0); }
DI void roleA1(const Params& p, LAS char* sm, int a) {
  const int xcd = a & 7, idx = a >> 3, rbl = idx & 7, cgl = idx >> 3;
  const int rb = (xcd >> 1) * 8 + rbl, cg = (xcd & 1) * 4 + cgl;
  const int w = rfl(threadIdx.x >> 6), l = threadIdx.x & 63, g = l >> 4, e = l & 15;
  const int head = cg * 4 + w;
  const u32 smb = (u32)(size_t)sm;
  RTDECL; RT(0);
  __builtin_amdgcn_s_setprio(2);
  const rsrc_t rx = mkrsrc(p.x, 2048 * 1024), rw = mkrsrc(p.wqkv, 512 * 3072);
  const u32 vx = (u32)((l >> 3) * 1024 + (((l & 7) ^ (l >> 3)) * 16));
  const u32 vw = (u32)((l >> 3) * 3072 + (((l & 7) ^ (((g & 1) | ((w & 1) << 1)) << 1)) * 16));
  const int s0 = (rbl + 2 * cgl) & 7;
  const u32 sxb = (u32)((rb * 64 + w * 8) * 1024), swb = (u32)(w * 8 * 3072 + cg * 128);
  auto dmaW = [&](auto S, auto PT) {
    constexpr int j = S.v + 4, pt = PT.v;
    if constexpr (j >= 0 && j <= 15)
      dma16<pt * 1024>(smb + A_W + (j & 3) * 12288 + w * 3072, rw, vw, swb + (u32)((((s0 + (j >> 1)) & 7) * 2 + (j & 1)) * (32 * 3072)));
  };
  auto dmaX = [&](auto S) {
    constexpr int s = S.v, m = (s + 5) >> 1, hf = (s + 5) & 1;
    if constexpr (s >= -5 && s <= 10)
      dma16<0>(smb + A_X + (m % 3) * 8192 + (hf * 4 + w) * 1024, rx, vx, sxb + (u32)(hf * 32 * 1024 + ((s0 + m) & 7) * 128));
  };
  sfor<-5, 0>([&](auto S) { dmaX(S); dmaW(S, IC<0>{}); dmaW(S, IC<1>{}); dmaW(S, IC<2>{}); });
  const u32 tag = sload(p.ws + WS_EPOCH + cg * 256) + 1u;
  const int xo = e * 128 + ((g ^ (l & 7)) * 16);
  const int q4 = e >> 2, pp = e & 3;
  const int wo_ = g * 3072 + q4 * 128 + ((w ^ (((q4 >> 1) & 1) | ((g & 1) << 1))) * 32) + pp * 8;
  LAS char* xp0 = sm + A_X + xo; LAS char* xp1 = sm + A_X + (xo ^ 64); LAS char* wp = sm + A_W + wo_;
  f32x4 aq[4], ak[4], av[4];
#pragma unroll
  for (int t = 0; t < 4; ++t) { aq[t] = (f32x4){0, 0, 0, 0}; ak[t] = aq[t]; av[t] = aq[t]; }
  bf16x8 xf[2][4], wf[2][3];
  auto rdX = [&](auto J, auto T) { constexpr int j = J.v, t = T.v;
    xf[j & 1][t] = *(LAS bf16x8*)(((j & 1) ? xp1 : xp0) + ((j >> 1) % 3) * 8192 + t * 2048); };
  auto rdW = [&](auto J, auto PT) { constexpr int j = J.v, pt = PT.v;
    wf[j & 1][pt] = cat8(trread(wp + (j & 3) * 12288 + pt * 1024), trread(wp + (j & 3) * 12288 + pt * 1024 + 512)); };
  wait_vm<ndma(-3) + ndma(-2) + ndma(-1)>();
  BAR();
  RT(5);
  sfor<0, 3>([&](auto PT) { rdW(IC<0>{}, PT); });
  sfor<0, 4>([&](auto T) { rdX(IC<0>{}, T); });
  sfor<0, 16>([&](auto J) {
    constexpr int j = J.v, c = j & 1;
    auto mm = [&](auto T) { constexpr int t = T.v;
      ak[t] = mfma32(xf[c][t], wf[c][1], ak[t]); av[t] = mfma32(xf[c][t], wf[c][2], av[t]); aq[t] = mfma32(wf[c][0], xf[c][t], aq[t]); };
    if constexpr (j < 15) {
      wait_vm<ndma(j - 2) + ndma(j - 1)>();
      wait_lgkm0();
      BAR();
      SB();
      rdW(IC<j + 1>{}, IC<1>{}); rdX(IC<j + 1>{}, IC<0>{}); rdW(IC<j + 1>{}, IC<2>{});
      dmaW(J, IC<0>{});
      SB();
      mm(IC<0>{});
      SB();
      rdW(IC<j + 1>{}, IC<0>{}); rdX(IC<j + 1>{}, IC<1>{});
      dmaW(J, IC<1>{});
      SB();
      mm(IC<1>{});
      SB();
      rdX(IC<j + 1>{}, IC<2>{}); rdX(IC<j + 1>{}, IC<3>{});
      dmaW(J, IC<2>{});
      SB();
      mm(IC<2>{});
      SB();
      dmaX(J);
      SB();
      mm(IC<3>{});
      SB();
    } else {
      sfor<0, 4>(mm);
    }
  });
  RT(1);
  f32x4 kv = {0, 0, 0, 0}, zz = {0, 0, 0, 0};
  const u32x4 ones = {0x3f803f80u, 0x3f803f80u, 0x3f803f80u, 0x3f803f80u};
#pragma unroll
  for (int h = 0; h < 2; ++h) {
    u32x4 kf = {pk2(phi(ak[2 * h][0]), phi(ak[2 * h][1])), pk2(phi(ak[2 * h][2]), phi(ak[2 * h][3])),
                pk2(phi(ak[2 * h + 1][0]), phi(ak[2 * h + 1][1])), pk2(phi(ak[2 * h + 1][2]), phi(ak[2 * h + 1][3]))};
    u32x4 vf = {pk2(av[2 * h][0], av[2 * h][1]), pk2(av[2 * h][2], av[2 * h][3]),
                pk2(av[2 * h + 1][0], av[2 * h + 1][1]), pk2(av[2 * h + 1][2], av[2 * h + 1][3])};
    kv = mfma32(as8(kf), as8(vf), kv);
    zz = mfma32(as8(kf), as8(ones), zz);
  }
  const rsrc_t rpart = mkrsrc(p.ws + WS_PART, 32 * 9 * 4096);
  st16_dev(rpart, (u32)(((head * 9 + (l >> 3)) * 32 + rb) * 128 + (l & 7) * 16), (u32x4){pk2(kv[0], kv[1]), tag, pk2(kv[2], kv[3]), tag});
  if (e < 2) st16_dev(rpart, (u32)(((head * 9 + 8) * 32 + rb) * 128 + (e * 4 + g) * 16), (u32x4){pk2(zz[0], zz[1]), tag, pk2(zz[2], zz[3]), tag});
  SB();
  RT(2);
  {
    char* pq = p.ws + WS_PQ + ((size_t)(rb * 2) * 32 + head) * 1024 + l * 16;
#pragma unroll
    for (int h = 0; h < 2; ++h)
      *(u32x4*)(pq + h * 32768) = (u32x4){pk2(phi(aq[2 * h][0]), phi(aq[2 * h][1])), pk2(phi(aq[2 * h][2]), phi(aq[2 * h][3])),
                                          pk2(phi(aq[2 * h + 1][0]), phi(aq[2 * h + 1][1])), pk2(phi(aq[2 * h + 1][2]), phi(aq[2 * h + 1][3]))};
  }
  RT(3);
  if (rb < 9) {
    const u32 off = (u32)((head * 9 + rb) * 4096 + l * 16);
    u32x4 v[4];
#pragma unroll 1
    for (int spin = 0; spin < SPIN_CAP; ++spin) {
      bool ok = true;
      asm volatile("" ::: "memory");
#pragma unroll
      for (int i = 0; i < 4; ++i) { v[i] = ld16_dev(rpart, off + i * 1024); ok &= (v[i][1] == tag) & (v[i][3] == tag); }
      if (__all(ok)) break;
      __builtin_amdgcn_s_sleep(1);
    }
    float s[4] = {0.f, 0.f, 0.f, 0.f};
#pragma unroll
    for (int i = 0; i < 4; ++i) { s[0] += bf2f(v[i][0]); s[1] += bfhi(v[i][0]); s[2] += bf2f(v[i][2]); s[3] += bfhi(v[i][2]); }
#pragma unroll
    for (int j = 0; j < 4; ++j) { s[j] += __shfl_xor(s[j], 8); s[j] += __shfl_xor(s[j], 16); s[j] += __shfl_xor(s[j], 32); }
    const u32x2 r = {pk2(s[0], s[1]), pk2(s[2], s[3])};
    if (rb < 8) { if (l < 8) *(u32x2*)(p.ws + WS_FIN + (head >> 1) * 1024 + (rb * 8 + l) * 16 + (head & 1) * 8) = r; }
    else if (l < 4) *(u32x2*)(p.ws + WS_FINZ + head * 32 + l * 8) = r;
    if (rb == 8) { BAR(); if (threadIdx.x == 0) __hip_atomic_store((GAS u32*)(p.ws + WS_EPOCH + cg * 256), tag, RLX_AGENT); }
  }
  RT(4);
  RTDUMP(0);
}

constexpr int B_SCR = 0;
constexpr int B_UT = 37120;
constexpr int B_UC = 46336;
constexpr int UC_S = 4160;
constexpr int B_KA = 62976;
constexpr int B_PX = 72192;
static_assert(B_PX + 8192 <= 81920, "lds");
DI void roleB1(const Params& p, LAS char* sm, int b) {
  const int gch = b & 7, bi = b >> 3;
  const int t = threadIdx.x, w = rfl(t >> 6), l = t & 63, g = l >> 4, e = l & 15;
  const int chl = w >> 1, mh = w & 1, ch = gch * 64 + bi * 2 + chl;
  RTDECL; RT(0);
  const char* ut = (const char*)p.u + (size_t)(bi * 64 + (t >> 3)) * 1024 + gch * 128 + (t & 7) * 16;
  const u32x4 u0 = *(const u32x4*)ut, u1 = *(const u32x4*)(ut + 32 * 1024);
  const int pi = ch * 128 + mh * 64 + l;
  const u32 r_lar = p.lar[pi], r_aim = p.aim[pi], r_cr = p.cr[pi], r_ci = p.ci[pi], r_ldt = p.ldt[ch];
  const u32 tag = sload(p.ws + WS_EPOCH + (8 + gch) * 256) + 1u;
  if (t < 64) *(LAS u32x4*)(sm + B_KA + (t >> 5) * 4608 + (t & 31) * 16) = (u32x4){0, 0, 0, 0};
  else if (t < 96) *(LAS u32*)(sm + B_UC + ((t - 64) >> 3) * UC_S + (t & 7) * 4) = 0u;
  *(LAS u32x4*)(sm + B_UT + (t >> 3) * 144 + (t & 7) * 16) = u0;
  *(LAS u32x4*)(sm + B_UT + ((t >> 3) + 32) * 144 + (t & 7) * 16) = u1;
  asm volatile("" :: "s"(tag) : "memory");
  LBAR();
  const rsrc_t rgu = mkrsrc(p.ws + WS_GU, 512 * 8192);
  {
    const int cq = w * 4 + g;
    u32x2 r[4];
#pragma unroll
    for (int j = 0; j < 4; ++j) r[j] = *(LAS u32x2*)(sm + B_UT + (e * 4 + j) * 144 + cq * 8);
    const u32 off = (u32)((gch * 64 + cq * 4) * 8192 + (bi * 16 + e) * 16);
#pragma unroll
    for (int c = 0; c < 4; ++c) {
      const u32 sel = (c & 1) ? 0x07060302u : 0x05040100u;
      const u32 d0 = __builtin_amdgcn_perm(r[1][c >> 1], r[0][c >> 1], sel), d1 = __builtin_amdgcn_perm(r[3][c >> 1], r[2][c >> 1], sel);
      st16_dev(rgu, off + c * 8192, (u32x4){d0, tag, d1, tag});
    }
  }
  SB();
  RT(1);
  const float dt = rbf(expf(bf2f(r_ldt)));
  const float a2 = -rbf(expf(bf2f(r_lar))) * dt * 1.44269504088896f;
  const float th = bf2f(r_aim) * dt * 0.15915494309189535f;
  auto wpow = [&](float s) -> f32x2 {
    const float m = __builtin_amdgcn_exp2f(a2 * s), f = __builtin_amdgcn_fractf(th * s);
    return (f32x2){m * __builtin_amdgcn_cosf(f), m * __builtin_amdgcn_sinf(f)};
  };
  const f32x2 w1 = wpow(1.f), w8 = wpow(8.f), w128 = wpow(128.f);
  const f32x2 w1n = {-w1.y, w1.y}, w128n = {-w128.y, w128.y}, w8cn = {w8.y, -w8.y};
  LAS char* scr = sm + B_SCR + w * 9248;
  LAS u32* sw = (LAS u32*)(scr + l * 4);
  const int fo = e * 272 + g * 16;
  auto rdf = [&](int reg, int sh, int ks) { return *(LAS bf16x8*)(scr + reg * 4624 + fo + sh * 272 + ks * 64); };
  u32 pc[17], pr[16];
  f32x2 G[8];
  {
    f32x2 c = {1.f, 0.f};
    pc[0] = 0x3f80u;
    G[0] = (f32x2){2.f * bf2f(r_cr) * dt, 2.f * bf2f(r_ci) * dt};
    pr[0] = pk2(G[0].x, G[0].y);
#pragma unroll
    for (int i = 1; i <= 16; ++i) {
      c = cmul(c, w8, w8cn); pc[i] = pk2(c.x, c.y);
      if (i < 8) { G[i] = cmul(G[i - 1], w1, w1n); pr[i] = pk2(G[i].x, G[i].y); }
      else if (i < 16) { G[i - 8] = cmul(G[i - 8], w128, w128n); pr[i] = pk2(G[i - 8].x, G[i - 8].y); }
    }
  }
  SB();
#pragma unroll
  for (int i = 0; i <= 16; ++i) sw[i * 68] = pc[i];
#pragma unroll
  for (int i = 0; i < 16; ++i) sw[1156 + i * 68] = pr[i];
  WFENCE();
  bf16x8 cf[4], cf2[4], af[4];
#pragma unroll
  for (int ks = 0; ks < 4; ++ks) { cf[ks] = rdf(0, 0, ks); af[ks] = rdf(1, 0, ks); cf2[ks] = rdf(0, 1, ks); }
  WFENCE();
  SB();
  u32 ps[16], pt_[8];
#pragma unroll
  for (int h = 0; h < 2; ++h)
#pragma unroll
    for (int i = 0; i < 4; ++i) { G[2 * i] = cmul(G[2 * i], w128, w128n); ps[4 * h + i] = pk2(G[2 * i].x, G[2 * i].y); }
#pragma unroll
  for (int h = 0; h < 4; ++h)
#pragma unroll
    for (int i = 0; i < 2; ++i) { G[4 * i] = cmul(G[4 * i], w128, w128n); ps[8 + 2 * h + i] = pk2(G[4 * i].x, G[4 * i].y); }
#pragma unroll
  for (int h = 0; h < 8; ++h) { G[0] = cmul(G[0], w128, w128n); pt_[h] = pk2(G[0].x, G[0].y); }
  SB();
#pragma unroll
  for (int i = 0; i < 16; ++i) sw[i * 68] = ps[i];
#pragma unroll
  for (int h = 0; h < 8; ++h) sw[1156 + (4 * (h >> 1) + (h & 1)) * 68] = pt_[h];
  WFENCE();
  bf16x8 a1[4], a2f[4];
#pragma unroll
  for (int ks = 0; ks < 4; ++ks) { a1[ks] = rdf(0, 0, ks); a2f[ks] = rdf(1, 0, ks); }
  const u32 uoff = (u32)(ch * 8192 + (mh * 256 + l) * 16);
  u32x4 v[4], vp;
#pragma unroll
  for (int i = 0; i < 4; ++i) v[i] = ld16_dev(rgu, uoff + i * 1024);
  vp = ld16_dev(rgu, (u32)(ch * 8192 + 255 * 16));
  SB();
  f32x4 d0 = {0, 0, 0, 0}, d1 = d0, d1s = d0, d2 = d0, d2s = d0;
#pragma unroll
  for (int ks = 0; ks < 4; ++ks) d0 = mfma32(af[ks], cf[ks], d0);
#pragma unroll
  for (int ks = 0; ks < 4; ++ks) {
    d1 = mfma32(a1[ks], cf[ks], d1); d1s = mfma32(a1[ks], cf2[ks], d1s);
    d2 = mfma32(a2f[ks], cf[ks], d2); d2s = mfma32(a2f[ks], cf2[ks], d2s);
  }
  {
    const f32x4 pa = {d2[0], d2[1], d2s[0], d2s[1]}, pb = {d1s[0], d1s[2], 0.f, 0.f};
    LAS f32x4* px = (LAS f32x4*)(sm + B_PX + w * 2048 + l * 16);
    px[0] = mh ? d0 : d1; px[64] = mh ? pa : pb;
    RT(2);
    LBAR();
    LAS f32x4* py = (LAS f32x4*)(sm + B_PX + (w ^ 1) * 2048 + l * 16);
    const f32x4 qa = py[0], qb = py[64];
    LAS char* ka = sm + B_KA + chl * 4608 + 512 + e * 16;
    if (mh == 0) {
      const f32x4 k0 = d0 + qa, k2 = pa + qb;
      *(LAS u32x2*)(ka + (g >> 1) * 256 + ((g & 1) ^ 1) * 8) = (u32x2){pk2(k0[3], k0[2]), pk2(k0[1], k0[0])};
#pragma unroll
      for (int r = 0; r < 2; ++r) {
        const float v_ = k2[r], w_ = k2[2 + r], m_ = (e & 1) ? w_ : v_;
        const u32 vv = pk2(v_, v_);
        *(LAS u32x4*)(ka + 2048 + (2 * g + r) * 256) = (u32x4){pk2(w_, w_), pk2(w_, m_), vv, vv};
      }
    } else {
      const f32x4 k1 = d1 + qa, k1s = pb + qb;
      const bool lo = g < 2;
      const float x_ = lo ? k1[2] : k1[1];
      *(LAS u32x4*)(ka + (lo ? 512 + g * 256 : 1024 + (g - 2) * 512)) =
          (u32x4){pk2(k1s[0], lo ? k1[3] : k1s[0]), pk2(x_, x_), pk2(x_, lo ? k1[1] : k1[0]), pk2(k1[0], k1[0])};
      if (!lo) *(LAS u32x4*)(ka + 1024 + (g - 2) * 512 + 256) = (u32x4){pk2(k1s[1], k1s[1]), pk2(k1[3], k1[3]), pk2(k1[3], k1[2]), pk2(k1[2], k1[2])};
    }
  }
#pragma unroll 1
  for (int spin = 0; spin < SPIN_CAP; ++spin) {
    bool ok = vp[3] == tag;
#pragma unroll
    for (int i = 0; i < 4; ++i) ok &= (v[i][1] == tag) & (v[i][3] == tag);
    if (__all(ok)) break;
    __builtin_amdgcn_s_sleep(1);
    asm volatile("" ::: "memory");
#pragma unroll
    for (int i = 0; i < 4; ++i) v[i] = ld16_dev(rgu, uoff + i * 1024);
    vp = ld16_dev(rgu, (u32)(ch * 8192 + 255 * 16));
  }
  RT(3);
  {
    LAS char* uc = sm + B_UC + chl * 2 * UC_S + 4 + 28 + (mh * 256 + l) * 8;
    u32 carry = mh ? vp[2] : 0u;
#pragma unroll
    for (int i = 0; i < 4; ++i) {
      const u32 prev = __builtin_amdgcn_update_dpp(carry, v[i][2], 0x138  , 0xf, 0xf, false);
      carry = __builtin_amdgcn_readlane(v[i][2], 63);
      *(LAS u32x2*)(uc + UC_S + i * 512) = (u32x2){v[i][0], v[i][2]};
      *(LAS u32x2*)(uc + i * 512) = (u32x2){__builtin_amdgcn_alignbit(v[i][0], prev, 16), __builtin_amdgcn_alignbit(v[i][2], v[i][0], 16)};
    }
  }
  LBAR();
  if (bi == 0 && t == 0) __hip_atomic_store((GAS u32*)(p.ws + WS_EPOCH + (8 + gch) * 256), tag, RLX_AGENT);
  RT(4);
  f32x4 acc[8];
#pragma unroll
  for (int c = 0; c < 8; ++c) acc[c] = (f32x4){0, 0, 0, 0};
  {
    LAS char* tb = sm + B_UC + (chl * 2 + (e & 1)) * UC_S + 4 + (16 * (g >> 1) - 8 * (g & 1) + 8 + (e & ~1)) * 2 + mh * 256;
    LAS char* vb = sm + B_KA + chl * 4608 + 512 + (e - (g >> 1)) * 32 + (g & 1) * 16 - mh * 256 - 192;
    bf16x8 T[2][8], V[2][8];
    auto rdT = [&](int ee, int m) { LAS u32* tp = (LAS u32*)(tb + ee * 64 + m * 512); T[ee & 1][m] = as8((u32x4){tp[0], tp[1], tp[2], tp[3]}); };
    auto rdV = [&](int ee, int m) { V[ee & 1][m] = *(LAS bf16x8*)(vb + (3 - ee) * 64 + m * 512); };
#pragma unroll
    for (int m = 0; m < 8; ++m) { rdT(0, m); rdV(0, m); }
    SB();
#pragma unroll
    for (int ee = 0; ee < 4; ++ee) {
#pragma unroll
      for (int s = 0; s < 8; ++s) {
        if (ee < 3) { rdT(ee + 1, s); rdV(ee + 1, s); }
#pragma unroll
        for (int m = 0; m <= s; ++m) {
          if (m + s < 8) acc[m + s] = mfma32(V[ee & 1][s], T[ee & 1][m], acc[m + s]);
          if (m < s && m + s < 8) acc[m + s] = mfma32(V[ee & 1][m], T[ee & 1][s], acc[m + s]);
        }
        SB();
      }
    }
  }
  RT(5);
#pragma unroll
  for (int c = 0; c < 8; ++c) *(LAS f32x4*)(sm + B_SCR + (w * 8 + c) * 1024 + l * 16) = acc[c];
  LBAR();
#pragma unroll
  for (int k = 0; k < 2; ++k) {
    const int c = 2 * w + k;
    LAS f32x4* xp = (LAS f32x4*)(sm + B_SCR + c * 1024 + l * 16);
    const f32x4 y0 = xp[0] + xp[512], y1 = xp[1024] + xp[1536];
    *(u32x4*)(p.ws + WS_YT + (size_t)(((gch * 32 + 4 * c + g) * 32 + bi) * 256 + e * 16)) =
        (u32x4){pk2(y0[0], y1[0]), pk2(y0[1], y1[1]), pk2(y0[2], y1[2]), pk2(y0[3], y1[3])};
  }
  RT(6);
  RTDUMP(0);
}

__global__ __launch_bounds__(256, 2) void k_one(Params p) {
  __shared__ __attribute__((aligned(16))) char smem[81920];
  if (blockIdx.x < 256) roleB1(p, (LAS char*)smem, blockIdx.x); else roleA1(p, (LAS char*)smem, blockIdx.x - 256);
}

constexpr int C_WO = 0, C_KV = 65536, C_ZX = 81920, C_YT = 86016, C_XC = 95232, C_ST = 111616, C_END = C_ST + 4 * 2304;
__global__ __launch_bounds__(256, 2) void k_two(Params p) {
  __shared__ __attribute__((aligned(16))) char smem[C_END];
  LAS char* sm = (LAS char*)smem;
  const int a = blockIdx.x, xcd = a & 7, idx = a >> 3;
  const int rb = (xcd >> 1) * 8 + (idx & 7), cg = (xcd & 1) * 4 + (idx >> 3);
  const int t = threadIdx.x, w = rfl(t >> 6), l = t & 63, g = l >> 4, e = l & 15;
  const int sh = w & 1, kh = w >> 1;
  const u32 smb = (u32)(size_t)sm;
  RTDECL; RT(0);
  const rsrc_t rfin = mkrsrc(p.ws + WS_FIN, 16384), rwo = mkrsrc(p.wo, 512 * 1024);
  sfor<0, 4>([&](auto I) { dma16<I.v * 1024>(smb + C_KV + w * 4096, rfin, (u32)(l * 16), (u32)(w * 4096)); });
  const u32x2 zr = *(const u32x2*)(p.ws + WS_FINZ + (t >> 3) * 32 + (t & 3) * 8);
  u32x4 pq[16];
  {
    const char* pp_ = p.ws + WS_PQ + (size_t)((rb * 2 + sh) * 32 + kh * 16) * 1024 + l * 16;
#pragma unroll
    for (int i = 0; i < 16; ++i) pq[i] = *(const u32x4*)(pp_ + i * 1024);
  }
  const char* yt = p.ws + WS_YT + (size_t)(xcd * 32 + idx) * 8192 + t * 16;
  const u32x4 y0 = *(const u32x4*)yt, y1 = *(const u32x4*)(yt + 4096);
  const char* ut = (const char*)p.u + (size_t)(idx * 64 + (t >> 3)) * 1024 + xcd * 128 + (t & 7) * 16;
  const u32x4 u0 = *(const u32x4*)ut, u1 = *(const u32x4*)(ut + 32 * 1024);
  const u32x4 dk = *(const u32x4*)((const char*)p.dsk + xcd * 128 + (t & 7) * 16);
  SB();
  {
    const u32 vw = (u32)((l >> 3) * 1024 + (((l & 7) ^ (g << 1)) * 16));
    sfor<0, 16>([&](auto I) { constexpr int hp = (I.v >> 1) + (I.v & 1) * 8;
      dma16<0>(smb + C_WO + (hp * 4 + w) * 1024, rwo, vw, (u32)(cg * 128 + (hp * 32 + w * 8) * 1024)); });
  }
  SB();
  wait_vm<16>();
  asm volatile("" : "+v"(*(u32x2*)&zr));
  *(LAS u32x4*)(sm + C_ZX + t * 16) = (t & 4) ? (u32x4){0, 0, zr[0], zr[1]} : (u32x4){zr[0], zr[1], 0, 0};
#pragma unroll
  for (int r = 0; r < 4; ++r) {
    *(LAS u32*)(sm + C_YT + (16 * r + e) * 144 + (t >> 4) * 4) = y0[r];
    *(LAS u32*)(sm + C_YT + (16 * r + e) * 144 + ((t >> 4) + 16) * 4) = y1[r];
  }
  LBAR();
  RT(1);
  const f32x4 zero = {0, 0, 0, 0};
  u32x4 of[2][8];
#pragma unroll
  for (int j = 0; j < 8; ++j) {
    const u32x4 kf = *(LAS u32x4*)(sm + C_KV + (kh * 8 + j) * 1024 + l * 16);
#pragma unroll
    for (int hh = 0; hh < 2; ++hh) {
      const u32x4 U = pq[2 * j + hh];
      const bf16x8 zf = *(LAS bf16x8*)(sm + C_ZX + (kh * 16 + 2 * j + hh) * 128 + ((e >> 1) & 1) * 64 + g * 16);
      const bf16x4 kh4 = as4((u32x2){kf[2 * hh], kf[2 * hh + 1]});
      const f32x4 na = mfma16(kh4, as4((u32x2){U[0], U[1]}), zero);
      const f32x4 nb = mfma16(kh4, as4((u32x2){U[2], U[3]}), zero);
      const f32x4 dn = mfma32(zf, as8(U), zero);
      const float ra = __builtin_amdgcn_rcpf(dn[0] + 1e-6f), rb_ = __builtin_amdgcn_rcpf(dn[2] + 1e-6f);
      of[0][j][2 * hh] = pk2(na[0] * ra, na[1] * ra); of[0][j][2 * hh + 1] = pk2(na[2] * ra, na[3] * ra);
      of[1][j][2 * hh] = pk2(nb[0] * rb_, nb[1] * rb_); of[1][j][2 * hh + 1] = pk2(nb[2] * rb_, nb[3] * rb_);
    }
  }
#pragma unroll
  for (int k = 0; k < 2; ++k) {
    const u32x4 yv = *(LAS u32x4*)(sm + C_YT + ((t >> 3) + 32 * k) * 144 + (t & 7) * 16);
    const u32x4 uv = k ? u1 : u0;
    u32x4 o;
#pragma unroll
    for (int j = 0; j < 4; ++j) {
      const u32 sk = pk2(bf2f(uv[j]) * bf2f(dk[j]), bfhi(uv[j]) * bfhi(dk[j]));
      o[j] = pk2(bf2f(yv[j]) + bf2f(sk), bfhi(yv[j]) + bfhi(sk));
    }
    *(u32x4*)((char*)p.out + (size_t)(idx * 64 + (t >> 3) + 32 * k) * 2048 + 1024 + xcd * 128 + (t & 7) * 16) = o;
  }
  SB();
  RT(2);
  wait_vm<2>();
  BAR();
  RT(3);
  f32x4 acc[2][4];
#pragma unroll
  for (int c = 0; c < 4; ++c) { acc[0][c] = zero; acc[1][c] = zero; }
  const int q4 = e >> 2, pp = e & 3, fw = (2 * g + (q4 >> 1)) & 3;
  LAS char* wbase = sm + C_WO + kh * 32768 + (4 * g + q4) * 128 + pp * 8;
  bf16x8 wf[2][4];
  auto rdw = [&](int j, int c) { LAS char* wq = wbase + ((c ^ fw) * 32) + j * 4096; wf[j & 1][c] = cat8(trread(wq), trread(wq + 2048)); };
#pragma unroll
  for (int c = 0; c < 4; ++c) rdw(0, c);
  SB();
#pragma unroll
  for (int j = 0; j < 8; ++j) {
#pragma unroll
    for (int c = 0; c < 4; ++c) {
      if (j < 7) rdw(j + 1, c);
      acc[0][c] = mfma32(wf[j & 1][c], as8(of[0][j]), acc[0][c]);
      acc[1][c] = mfma32(wf[j & 1][c], as8(of[1][j]), acc[1][c]);
      SB();
    }
  }
  RT(4);
  f32x4 mine[4];
#pragma unroll
  for (int c = 0; c < 4; ++c) {
    *(LAS f32x4*)(sm + C_XC + (w * 4 + c) * 1024 + l * 16) = kh ? acc[0][c] : acc[1][c];
    mine[c] = kh ? acc[1][c] : acc[0][c];
  }
  LBAR();
  LAS char* st = sm + C_ST + w * 2304;
#pragma unroll
  for (int c = 0; c < 4; ++c) {
    const f32x4 s = mine[c] + *(LAS f32x4*)(sm + C_XC + ((w ^ 2) * 4 + c) * 1024 + l * 16);
    *(LAS u32x2*)(st + e * 144 + c * 32 + g * 8) = (u32x2){pk2(s[0], s[1]), pk2(s[2], s[3])};
  }
  WFENCE();
#pragma unroll
  for (int k = 0; k < 2; ++k) {
    const int row = (l >> 3) + 8 * k;
    *(u32x4*)((char*)p.out + (size_t)(rb * 64 + (sh * 2 + kh) * 16 + row) * 2048 + cg * 128 + (l & 7) * 16) = *(LAS u32x4*)(st + row * 144 + (l & 7) * 16);
  }
  RT(5);
  RTDUMP(1);
}

__device__ float diag_stat(const u64* st, u64 t0, int k, int half, int slot, int mode, float* red) {
  float mn = 1e30f, mx = -1e30f, sum = 0.f, cnt = 0.f;
  for (int i = threadIdx.x; i < 256 * DIAGW; i += 256) {
    const int b = half * 256 + i / DIAGW, w = i % DIAGW;
    const u64 v = st[((size_t)(k * 512 + b) * 8 + w) * 16 + slot];
    if (v >= t0 && v - t0 < 100000) { const float d = (float)(v - t0); mn = fminf(mn, d); mx = fmaxf(mx, d); sum += d; cnt += 1.f; }
  }
  __syncthreads();
  red[threadIdx.x] = mn; red[256 + threadIdx.x] = mx; red[512 + threadIdx.x] = sum; red[768 + threadIdx.x] = cnt;
  __syncthreads();
  if (threadIdx.x == 0) for (int i = 1; i < 256; ++i) { red[0] = fminf(red[0], red[i]); red[256] = fmaxf(red[256], red[256 + i]); red[512] += red[512 + i]; red[768] += red[768 + i]; }
  __syncthreads();
  return mode == 0 ? red[0] : mode == 1 ? red[512] / fmaxf(red[768], 1.f) : red[256];
}
__global__ void k_diag(char* ws) {
  __shared__ float red[1024];
  __shared__ u64 r64[256];
  const u64 tstart = __builtin_amdgcn_s_memrealtime();
  const u64* st = (const u64*)(ws + WS_DBG);
  u64 m = ~0ull;
  for (int i = threadIdx.x; i < 512 * DIAGW; i += 256) { const u64 v = st[((size_t)(i / DIAGW) * 8 + i % DIAGW) * 16]; if (v <= tstart && tstart - v < 100000) m = v < m ? v : m; }
  r64[threadIdx.x] = m; __syncthreads();
  if (threadIdx.x == 0) for (int i = 1; i < 256; ++i) r64[0] = r64[i] < r64[0] ? r64[i] : r64[0];
  __syncthreads();
  const u64 t0 = r64[0];
  const float s1 = diag_stat(st, t0, D1K, D1H, D1S, D1M, red), s2 = diag_stat(st, t0, D2K, D2H, D2S, D2M, red);
  const u64 q1 = (u64)fminf(s1 / 10.f + .5f, 299.f), q2 = (u64)fminf(s2 / 10.f + .5f, 299.f);
  const u64 until = t0 + 10000 + q1 * 100 + q2 * 30000;
  while (__builtin_amdgcn_s_memrealtime() < until) __builtin_amdgcn_s_sleep(8);
}
extern "C" void kernel_launch(void* const* d_in, const int* in_sizes, int n_in, void* d_out, int out_size, void* d_ws, size_t ws_size,
                              hipStream_t stream) {
  Params p;
  p.x = (const u16*)d_in[0]; p.wqkv = (const u16*)d_in[1]; p.wo = (const u16*)d_in[2]; p.u = (const u16*)d_in[3];
  p.lar = (const u16*)d_in[4]; p.aim = (const u16*)d_in[5]; p.cr = (const u16*)d_in[6]; p.ci = (const u16*)d_in[7];
  p.ldt = (const u16*)d_in[8]; p.dsk = (const u16*)d_in[9]; p.out = (u16*)d_out; p.ws = (char*)d_ws;
  k_one<<<512, 256, 0, stream>>>(p);
  k_two<<<256, 256, 0, stream>>>(p);
  k_diag<<<1, 256, 0, stream>>>(p.ws);
}
```

```cpp
#define STAMP
#include <hip/hip_runtime.h>
#include <hip/hip_bf16.h>

typedef unsigned short u16;
typedef unsigned int u32;
typedef unsigned long long u64;
typedef short bf16x8 __attribute__((ext_vector_type(8)));
typedef short bf16x4 __attribute__((ext_vector_type(4)));
typedef float f32x4 __attribute__((ext_vector_type(4)));
typedef float f32x2 __attribute__((ext_vector_type(2)));
typedef u32 u32x4 __attribute__((ext_vector_type(4)));
typedef u32 u32x2 __attribute__((ext_vector_type(2)));
typedef __bf16 bfv2 __attribute__((ext_vector_type(2)));
typedef __amdgpu_buffer_rsrc_t rsrc_t;
#define GAS __attribute__((address_space(1)))
#define LAS __attribute__((address_space(3)))
#define DI __device__ __forceinline__
#define RLX_AGENT __ATOMIC_RELAXED, __HIP_MEMORY_SCOPE_AGENT
#define SB() __builtin_amdgcn_sched_barrier(0)

#define STAMP_AT(k, i) do { if ((threadIdx.x & 63) == 0) ((u64*)(p.ws + WS_DBG))[(((k) * 512 + blockIdx.x) * 4 + (threadIdx.x >> 6)) * 16 + (i)] = __builtin_amdgcn_s_memrealtime(); } while (0)
#define RTDECL u64 rtk[12] = {0,0,0,0,0,0,0,0,0,0,0,0}, ctk[12] = {0,0,0,0,0,0,0,0,0,0,0,0}
#define RT(i) do { SB(); asm volatile("s_memrealtime %0\n\ts_memtime %1\n\ts_waitcnt lgkmcnt(0)" : "=s"(rtk[i]), "=s"(ctk[i]) :: "memory"); SB(); } while (0)
#define RTDUMP(k) do { if ((threadIdx.x & 63) == 0) for (int i_ = 0; i_ < 12; ++i_) { ((u64*)(p.ws + WS_DBG))[(((k) * 512 + blockIdx.x) * 8 + (threadIdx.x >> 6)) * 16 + i_] = rtk[i_]; ((u64*)(p.ws + WS_DBG + (4u << 20)))[(((k) * 512 + blockIdx.x) * 8 + (threadIdx.x >> 6)) * 16 + i_] = ctk[i_]; } } while (0)

DI float bf2f(u32 h) { return __uint_as_float(h << 16); }
DI float bfhi(u32 h) { return __uint_as_float(h & 0xffff0000u); }
DI u32 pk2(float lo, float hi) { bfv2 v = __builtin_convertvector((f32x2){lo, hi}, bfv2); return __builtin_bit_cast(u32, v); }
DI float rbf(float x) { return bf2f(pk2(x, x) & 0xffffu); }
DI f32x4 mfma32(bf16x8 a, bf16x8 b, f32x4 c) { return __builtin_amdgcn_mfma_f32_16x16x32_bf16(a, b, c, 0, 0, 0); }
DI f32x4 mfma16(bf16x4 a, bf16x4 b, f32x4 c) { return __builtin_amdgcn_mfma_f32_16x16x16bf16_1k(a, b, c, 0, 0, 0); }
DI bf16x8 as8(u32x4 v) { return __builtin_bit_cast(bf16x8, v); }
DI bf16x4 as4(u32x2 v) { return __builtin_bit_cast(bf16x4, v); }
DI u32 rfl(u32 v) { return __builtin_amdgcn_readfirstlane(v); }
DI rsrc_t mkrsrc(const void* base, u32 bytes) { return __builtin_amdgcn_make_buffer_rsrc((void*)base, 0, bytes, 0x00020000); }
template <int OFF> DI void dma16(u32 m0v, rsrc_t r, u32 voff, u32 soff) {
  asm volatile("s_mov_b32 m0, %0\n\ts_nop 0\n\tbuffer_load_dwordx4 %1, %2, %3 offen offset:%4 lds"
               :: "s"(m0v), "v"(voff), "s"(r), "s"(soff), "i"(OFF) : "memory");
}
template <int N> DI void wait_vm() { asm volatile("s_waitcnt vmcnt(%0)" :: "i"(N) : "memory"); }
DI void wait_lgkm0() { __builtin_amdgcn_s_waitcnt(0xC07F); asm volatile("" ::: "memory"); }
#define BAR() do { asm volatile("" ::: "memory"); __builtin_amdgcn_s_barrier(); asm volatile("" ::: "memory"); } while (0)
#define LBAR() do { wait_lgkm0(); BAR(); } while (0)
#define WFENCE() do { __builtin_amdgcn_wave_barrier(); asm volatile("" ::: "memory"); } while (0)
DI void st16_dev(rsrc_t r, u32 off, u32x4 v) { __builtin_amdgcn_raw_buffer_store_b128(v, r, off, 0, 16); }
DI u32x4 ld16_dev(rsrc_t r, u32 off) { return __builtin_amdgcn_raw_buffer_load_b128(r, off, 0, 16); }
DI bf16x4 trread(LAS char* a) { return __builtin_amdgcn_ds_read_tr16_b64_v4i16((LAS bf16x4*)a); }
DI bf16x8 cat8(bf16x4 a, bf16x4 b) { return __builtin_shufflevector(a, b, 0, 1, 2, 3, 4, 5, 6, 7); }
DI float phi(float t) { float e = __builtin_amdgcn_exp2f(t * 1.44269504088896f); return (t > 0.f ? t + 1.f : e) + 1e-6f; }
DI f32x2 cmul(f32x2 x, f32x2 w, f32x2 wn) {
  f32x2 t = x * (f32x2){w.x, w.x};
  return __builtin_elementwise_fma((f32x2){x.y, x.x}, wn, t);
}
template <int I> struct IC { static constexpr int v = I; };
template <int B, int E, class F> DI void sfor(F&& f) { if constexpr (B < E) { f(IC<B>{}); sfor<B + 1, E>(f); } }

constexpr size_t WS_EPOCH = 0;
constexpr size_t WS_FIN   = 8192;
constexpr size_t WS_FINZ  = WS_FIN + 16384;
constexpr size_t WS_PART  = 65536;
constexpr size_t WS_PQ    = 2u << 20;
constexpr size_t WS_GU    = 4u << 20;
constexpr size_t WS_YT    = 8u << 20;
constexpr size_t WS_DBG   = 16u << 20;
constexpr int SPIN_CAP = 4096;

struct Params {
  const u16 *x, *wqkv, *wo, *u, *lar, *aim, *cr, *ci, *ldt, *dsk;
  u16* out;
  char* ws;
};
DI u32 sload(const void* p) { return *(const __attribute__((address_space(4))) u32*)p; }

constexpr int A_W = 0, A_X = 49152;
constexpr int ndma(int s) { return (s + 4 <= 15 && s >= -4 ? 3 : 0) + (s >= -5 && s <= 10 ? 1 : 0); }
DI void roleA1(const Params& p, LAS char* sm, int a) {
  const int xcd = a & 7, idx = a >> 3, rbl = idx & 7, cgl = idx >> 3;
  const int rb = (xcd >> 1) * 8 + rbl, cg = (xcd & 1) * 4 + cgl;
  const int w = rfl(threadIdx.x >> 6), l = threadIdx.x & 63, g = l >> 4, e = l & 15;
  const int head = cg * 4 + w;
  const u32 smb = (u32)(size_t)sm;
  RTDECL; RT(0);
  __builtin_amdgcn_s_setprio(2);
  const rsrc_t rx = mkrsrc(p.x, 2048 * 1024), rw = mkrsrc(p.wqkv, 512 * 3072);
  const u32 vx = (u32)((l >> 3) * 1024 + (((l & 7) ^ (l >> 3)) * 16));
  const u32 vw = (u32)((l >> 3) * 3072 + (((l & 7) ^ (((g & 1) | ((w & 1) << 1)) << 1)) * 16));
  const int s0 = (rbl + 2 * cgl) & 7;
  const u32 sxb = (u32)((rb * 64 + w * 8) * 1024), swb = (u32)(w * 8 * 3072 + cg * 128);
  auto dmaW = [&](auto S, auto PT) {
    constexpr int j = S.v + 4, pt = PT.v;
    if constexpr (j >= 0 && j <= 15)
      dma16<pt * 1024>(smb + A_W + (j & 3) * 12288 + w * 3072, rw, vw, swb + (u32)((((s0 + (j >> 1)) & 7) * 2 + (j & 1)) * (32 * 3072)));
  };
  auto dmaX = [&](auto S) {
    constexpr int s = S.v, m = (s + 5) >> 1, hf = (s + 5) & 1;
    if constexpr (s >= -5 && s <= 10)
      dma16<0>(smb + A_X + (m % 3) * 8192 + (hf * 4 + w) * 1024, rx, vx, sxb + (u32)(hf * 32 * 1024 + ((s0 + m) & 7) * 128));
  };
  sfor<-5, 0>([&](auto S) { dmaX(S); dmaW(S, IC<0>{}); dmaW(S, IC<1>{}); dmaW(S, IC<2>{}); });
  const u32 tag = sload(p.ws + WS_EPOCH + cg * 256) + 1u;
  const int xo = e * 128 + ((g ^ (l & 7)) * 16);
  const int q4 = e >> 2, pp = e & 3;
  const int wo_ = g * 3072 + q4 * 128 + ((w ^ (((q4 >> 1) & 1) | ((g & 1) << 1))) * 32) + pp * 8;
  LAS char* xp0 = sm + A_X + xo; LAS char* xp1 = sm + A_X + (xo ^ 64); LAS char* wp = sm + A_W + wo_;
  f32x4 aq[4], ak[4], av[4];
#pragma unroll
  for (int t = 0; t < 4; ++t) { aq[t] = (f32x4){0, 0, 0, 0}; ak[t] = aq[t]; av[t] = aq[t]; }
  bf16x8 xf[2][4], wf[2][3];
  auto rdX = [&](auto J, auto T) { constexpr int j = J.v, t = T.v;
    xf[j & 1][t] = *(LAS bf16x8*)(((j & 1) ? xp1 : xp0) + ((j >> 1) % 3) * 8192 + t * 2048); };
  auto rdW = [&](auto J, auto PT) { constexpr int j = J.v, pt = PT.v;
    wf[j & 1][pt] = cat8(trread(wp + (j & 3) * 12288 + pt * 1024), trread(wp + (j & 3) * 12288 + pt * 1024 + 512)); };
  wait_vm<ndma(-3) + ndma(-2) + ndma(-1)>();
  BAR();
  RT(5);
  sfor<0, 3>([&](auto PT) { rdW(IC<0>{}, PT); });
  sfor<0, 4>([&](auto T) { rdX(IC<0>{}, T); });
  sfor<0, 16>([&](auto J) {
    constexpr int j = J.v, c = j & 1;
    auto mm = [&](auto T) { constexpr int t = T.v;
      ak[t] = mfma32(xf[c][t], wf[c][1], ak[t]); av[t] = mfma32(xf[c][t], wf[c][2], av[t]); aq[t] = mfma32(wf[c][0], xf[c][t], aq[t]); };
    if constexpr (j < 15) {
      wait_vm<ndma(j - 2) + ndma(j - 1)>();
      wait_lgkm0();
      BAR();
      SB();
      rdW(IC<j + 1>{}, IC<1>{}); rdX(IC<j + 1>{}, IC<0>{}); rdW(IC<j + 1>{}, IC<2>{});
      dmaW(J, IC<0>{});
      SB();
      mm(IC<0>{});
      SB();
      rdW(IC<j + 1>{}, IC<0>{}); rdX(IC<j + 1>{}, IC<1>{});
      dmaW(J, IC<1>{});
      SB();
      mm(IC<1>{});
      SB();
      rdX(IC<j + 1>{}, IC<2>{}); rdX(IC<j + 1>{}, IC<3>{});
      dmaW(J, IC<2>{});
      SB();
      mm(IC<2>{});
      SB();
      dmaX(J);
      SB();
      mm(IC<3>{});
      SB();
    } else {
      sfor<0, 4>(mm);
    }
  });
  RT(1);
  f32x4 kv = {0, 0, 0, 0}, zz = {0, 0, 0, 0};
  const u32x4 ones = {0x3f803f80u, 0x3f803f80u, 0x3f803f80u, 0x3f803f80u};
#pragma unroll
  for (int h = 0; h < 2; ++h) {
    u32x4 kf = {pk2(phi(ak[2 * h][0]), phi(ak[2 * h][1])), pk2(phi(ak[2 * h][2]), phi(ak[2 * h][3])),
                pk2(phi(ak[2 * h + 1][0]), phi(ak[2 * h + 1][1])), pk2(phi(ak[2 * h + 1][2]), phi(ak[2 * h + 1][3]))};
    u32x4 vf = {pk2(av[2 * h][0], av[2 * h][1]), pk2(av[2 * h][2], av[2 * h][3]),
                pk2(av[2 * h + 1][0], av[2 * h + 1][1]), pk2(av[2 * h + 1][2], av[2 * h + 1][3])};
    kv = mfma32(as8(kf), as8(vf), kv);
    zz = mfma32(as8(kf), as8(ones), zz);
  }
  const rsrc_t rpart = mkrsrc(p.ws + WS_PART, 32 * 9 * 4096);
  st16_dev(rpart, (u32)(((head * 9 + (l >> 3)) * 32 + rb) * 128 + (l & 7) * 16), (u32x4){pk2(kv[0], kv[1]), tag, pk2(kv[2], kv[3]), tag});
  if (e < 2) st16_dev(rpart, (u32)(((head * 9 + 8) * 32 + rb) * 128 + (e * 4 + g) * 16), (u32x4){pk2(zz[0], zz[1]), tag, pk2(zz[2], zz[3]), tag});
  SB();
  RT(2);
  {
    char* pq = p.ws + WS_PQ + ((size_t)(rb * 2) * 32 + head) * 1024 + l * 16;
#pragma unroll
    for (int h = 0; h < 2; ++h)
      *(u32x4*)(pq + h * 32768) = (u32x4){pk2(phi(aq[2 * h][0]), phi(aq[2 * h][1])), pk2(phi(aq[2 * h][2]), phi(aq[2 * h][3])),
                                          pk2(phi(aq[2 * h + 1][0]), phi(aq[2 * h + 1][1])), pk2(phi(aq[2 * h + 1][2]), phi(aq[2 * h + 1][3]))};
  }
  RT(3);
  if (rb < 9) {
    const u32 off = (u32)((head * 9 + rb) * 4096 + l * 16);
    u32x4 v[4];
#pragma unroll 1
    for (int spin = 0; spin < SPIN_CAP; ++spin) {
      bool ok = true;
      asm volatile("" ::: "memory");
#pragma unroll
      for (int i = 0; i < 4; ++i) { v[i] = ld16_dev(rpart, off + i * 1024); ok &= (v[i][1] == tag) & (v[i][3] == tag); }
      if (__all(ok)) break;
      __builtin_amdgcn_s_sleep(1);
    }
    float s[4] = {0.f, 0.f, 0.f, 0.f};
#pragma unroll
    for (int i = 0; i < 4; ++i) { s[0] += bf2f(v[i][0]); s[1] += bfhi(v[i][0]); s[2] += bf2f(v[i][2]); s[3] += bfhi(v[i][2]); }
#pragma unroll
    for (int j = 0; j < 4; ++j) { s[j] += __shfl_xor(s[j], 8); s[j] += __shfl_xor(s[j], 16); s[j] += __shfl_xor(s[j], 32); }
    const u32x2 r = {pk2(s[0], s[1]), pk2(s[2], s[3])};
    if (rb < 8) { if (l < 8) *(u32x2*)(p.ws + WS_FIN + (head >> 1) * 1024 + (rb * 8 + l) * 16 + (head & 1) * 8) = r; }
    else if (l < 4) *(u32x2*)(p.ws + WS_FINZ + head * 32 + l * 8) = r;
    if (rb == 8) { BAR(); if (threadIdx.x == 0) __hip_atomic_store((GAS u32*)(p.ws + WS_EPOCH + cg * 256), tag, RLX_AGENT); }
  }
  RT(4);
  RTDUMP(0);
}

constexpr int B_SCR = 0;
constexpr int B_UT = 37120;
constexpr int B_UC = 46336;
constexpr int UC_S = 4160;
constexpr int B_KA = 62976;
constexpr int B_PX = 72192;
static_assert(B_PX + 8192 <= 81920, "lds");
DI void roleB1(const Params& p, LAS char* sm, int b) {
  const int gch = b & 7, bi = b >> 3;
  const int t = threadIdx.x, w = rfl(t >> 6), l = t & 63, g = l >> 4, e = l & 15;
  const int chl = w >> 1, mh = w & 1, ch = gch * 64 + bi * 2 + chl;
  RTDECL; RT(0);
  const char* ut = (const char*)p.u + (size_t)(bi * 64 + (t >> 3)) * 1024 + gch * 128 + (t & 7) * 16;
  const u32x4 u0 = *(const u32x4*)ut, u1 = *(const u32x4*)(ut + 32 * 1024);
  const int pi = ch * 128 + mh * 64 + l;
  const u32 r_lar = p.lar[pi], r_aim = p.aim[pi], r_cr = p.cr[pi], r_ci = p.ci[pi], r_ldt = p.ldt[ch];
  const u32 tag = sload(p.ws + WS_EPOCH + (8 + gch) * 256) + 1u;
  if (t < 64) *(LAS u32x4*)(sm + B_KA + (t >> 5) * 4608 + (t & 31) * 16) = (u32x4){0, 0, 0, 0};
  else if (t < 96) *(LAS u32*)(sm + B_UC + ((t - 64) >> 3) * UC_S + (t & 7) * 4) = 0u;
  *(LAS u32x4*)(sm + B_UT + (t >> 3) * 144 + (t & 7) * 16) = u0;
  *(LAS u32x4*)(sm + B_UT + ((t >> 3) + 32) * 144 + (t & 7) * 16) = u1;
  asm volatile("" :: "s"(tag) : "memory");
  LBAR();
  const rsrc_t rgu = mkrsrc(p.ws + WS_GU, 512 * 8192);
  {
    const int cq = w * 4 + g;
    u32x2 r[4];
#pragma unroll
    for (int j = 0; j < 4; ++j) r[j] = *(LAS u32x2*)(sm + B_UT + (e * 4 + j) * 144 + cq * 8);
    const u32 off = (u32)((gch * 64 + cq * 4) * 8192 + (bi * 16 + e) * 16);
#pragma unroll
    for (int c = 0; c < 4; ++c) {
      const u32 sel = (c & 1) ? 0x07060302u : 0x05040100u;
      const u32 d0 = __builtin_amdgcn_perm(r[1][c >> 1], r[0][c >> 1], sel), d1 = __builtin_amdgcn_perm(r[3][c >> 1], r[2][c >> 1], sel);
      st16_dev(rgu, off + c * 8192, (u32x4){d0, tag, d1, tag});
    }
  }
  SB();
  RT(1);
  const float dt = rbf(expf(bf2f(r_ldt)));
  const float a2 = -rbf(expf(bf2f(r_lar))) * dt * 1.44269504088896f;
  const float th = bf2f(r_aim) * dt * 0.15915494309189535f;
  auto wpow = [&](float s) -> f32x2 {
    const float m = __builtin_amdgcn_exp2f(a2 * s), f = __builtin_amdgcn_fractf(th * s);
    return (f32x2){m * __builtin_amdgcn_cosf(f), m * __builtin_amdgcn_sinf(f)};
  };
  const f32x2 w1 = wpow(1.f), w8 = wpow(8.f), w128 = wpow(128.f);
  const f32x2 w1n = {-w1.y, w1.y}, w128n = {-w128.y, w128.y}, w8cn = {w8.y, -w8.y};
  LAS char* scr = sm + B_SCR + w * 9248;
  LAS u32* sw = (LAS u32*)(scr + l * 4);
  const int fo = e * 272 + g * 16;
  auto rdf = [&](int reg, int sh, int ks) { return *(LAS bf16x8*)(scr + reg * 4624 + fo + sh * 272 + ks * 64); };
  u32 pc[17], pr[16];
  f32x2 G[8];
  {
    f32x2 c = {1.f, 0.f};
    pc[0] = 0x3f80u;
    G[0] = (f32x2){2.f * bf2f(r_cr) * dt, 2.f * bf2f(r_ci) * dt};
    pr[0] = pk2(G[0].x, G[0].y);
#pragma unroll
    for (int i = 1; i <= 16; ++i) {
      c = cmul(c, w8, w8cn); pc[i] = pk2(c.x, c.y);
      if (i < 8) { G[i] = cmul(G[i - 1], w1, w1n); pr[i] = pk2(G[i].x, G[i].y); }
      else if (i < 16) { G[i - 8] = cmul(G[i - 8], w128, w128n); pr[i] = pk2(G[i - 8].x, G[i - 8].y); }
    }
  }
  SB();
#pragma unroll
  for (int i = 0; i <= 16; ++i) sw[i * 68] = pc[i];
#pragma unroll
  for (int i = 0; i < 16; ++i) sw[1156 + i * 68] = pr[i];
  WFENCE();
  bf16x8 cf[4], cf2[4], af[4];
#pragma unroll
  for (int ks = 0; ks < 4; ++ks) { cf[ks] = rdf(0, 0, ks); af[ks] = rdf(1, 0, ks); cf2[ks] = rdf(0, 1, ks); }
  WFENCE();
  SB();
  u32 ps[16], pt_[8];
#pragma unroll
  for (int h = 0; h < 2; ++h)
#pragma unroll
    for (int i = 0; i < 4; ++i) { G[2 * i] = cmul(G[2 * i], w128, w128n); ps[4 * h + i] = pk2(G[2 * i].x, G[2 * i].y); }
#pragma unroll
  for (int h = 0; h < 4; ++h)
#pragma unroll
    for (int i = 0; i < 2; ++i) { G[4 * i] = cmul(G[4 * i], w128, w128n); ps[8 + 2 * h + i] = pk2(G[4 * i].x, G[4 * i].y); }
#pragma unroll
  for (int h = 0; h < 8; ++h) { G[0] = cmul(G[0], w128, w128n); pt_[h] = pk2(G[0].x, G[0].y); }
  SB();
#pragma unroll
  for (int i = 0; i < 16; ++i) sw[i * 68] = ps[i];
#pragma unroll
  for (int h = 0; h < 8; ++h) sw[1156 + (4 * (h >> 1) + (h & 1)) * 68] = pt_[h];
  WFENCE();
  bf16x8 a1[4], a2f[4];
#pragma unroll
  for (int ks = 0; ks < 4; ++ks) { a1[ks] = rdf(0, 0, ks); a2f[ks] = rdf(1, 0, ks); }
  const u32 uoff = (u32)(ch * 8192 + (mh * 256 + l) * 16);
  u32x4 v[4], vp;
#pragma unroll
  for (int i = 0; i < 4; ++i) v[i] = ld16_dev(rgu, uoff + i * 1024);
  vp = ld16_dev(rgu, (u32)(ch * 8192 + 255 * 16));
  SB();
  f32x4 d0 = {0, 0, 0, 0}, d1 = d0, d1s = d0, d2 = d0, d2s = d0;
#pragma unroll
  for (int ks = 0; ks < 4; ++ks) d0 = mfma32(af[ks], cf[ks], d0);
#pragma unroll
  for (int ks = 0; ks < 4; ++ks) {
    d1 = mfma32(a1[ks], cf[ks], d1); d1s = mfma32(a1[ks], cf2[ks], d1s);
    d2 = mfma32(a2f[ks], cf[ks], d2); d2s = mfma32(a2f[ks], cf2[ks], d2s);
  }
  {
    const f32x4 pa = {d2[0], d2[1], d2s[0], d2s[1]}, pb = {d1s[0], d1s[2], 0.f, 0.f};
    LAS f32x4* px = (LAS f32x4*)(sm + B_PX + w * 2048 + l * 16);
    px[0] = mh ? d0 : d1; px[64] = mh ? pa : pb;
    RT(2);
    LBAR();
    LAS f32x4* py = (LAS f32x4*)(sm + B_PX + (w ^ 1) * 2048 + l * 16);
    const f32x4 qa = py[0], qb = py[64];
    LAS char* ka = sm + B_KA + chl * 4608 + 512 + e * 16;
    if (mh == 0) {
      const f32x4 k0 = d0 + qa, k2 = pa + qb;
      *(LAS u32x2*)(ka + (g >> 1) * 256 + ((g & 1) ^ 1) * 8) = (u32x2){pk2(k0[3], k0[2]), pk2(k0[1], k0[0])};
#pragma unroll
      for (int r = 0; r < 2; ++r) {
        const float v_ = k2[r], w_ = k2[2 + r], m_ = (e & 1) ? w_ : v_;
        const u32 vv = pk2(v_, v_);
        *(LAS u32x4*)(ka + 2048 + (2 * g + r) * 256) = (u32x4){pk2(w_, w_), pk2(w_, m_), vv, vv};
      }
    } else {
      const f32x4 k1 = d1 + qa, k1s = pb + qb;
      const bool lo = g < 2;
      const float x_ = lo ? k1[2] : k1[1];
      *(LAS u32x4*)(ka + (lo ? 512 + g * 256 : 1024 + (g - 2) * 512)) =
          (u32x4){pk2(k1s[0], lo ? k1[3] : k1s[0]), pk2(x_, x_), pk2(x_, lo ? k1[1] : k1[0]), pk2(k1[0], k1[0])};
      if (!lo) *(LAS u32x4*)(ka + 1024 + (g - 2) * 512 + 256) = (u32x4){pk2(k1s[1], k1s[1]), pk2(k1[3], k1[3]), pk2(k1[3], k1[2]), pk2(k1[2], k1[2])};
    }
  }
#pragma unroll 1
  for (int spin = 0; spin < SPIN_CAP; ++spin) {
    bool ok = vp[3] == tag;
#pragma unroll
    for (int i = 0; i < 4; ++i) ok &= (v[i][1] == tag) & (v[i][3] == tag);
    if (__all(ok)) break;
    __builtin_amdgcn_s_sleep(1);
    asm volatile("" ::: "memory");
#pragma unroll
    for (int i = 0; i < 4; ++i) v[i] = ld16_dev(rgu, uoff + i * 1024);
    vp = ld16_dev(rgu, (u32)(ch * 8192 + 255 * 16));
  }
  RT(3);
  {
    LAS char* uc = sm + B_UC + chl * 2 * UC_S + 4 + 28 + (mh * 256 + l) * 8;
    u32 carry = mh ? vp[2] : 0u;
#pragma unroll
    for (int i = 0; i < 4; ++i) {
      const u32 prev = __builtin_amdgcn_update_dpp(carry, v[i][2], 0x138  , 0xf, 0xf, false);
      carry = __builtin_amdgcn_readlane(v[i][2], 63);
      *(LAS u32x2*)(uc + UC_S + i * 512) = (u32x2){v[i][0], v[i][2]};
      *(LAS u32x2*)(uc + i * 512) = (u32x2){__builtin_amdgcn_alignbit(v[i][0], prev, 16), __builtin_amdgcn_alignbit(v[i][2], v[i][0], 16)};
    }
  }
  LBAR();
  if (bi == 0 && t == 0) __hip_atomic_store((GAS u32*)(p.ws + WS_EPOCH + (8 + gch) * 256), tag, RLX_AGENT);
  RT(4);
  f32x4 acc[8];
#pragma unroll
  for (int c = 0; c < 8; ++c) acc[c] = (f32x4){0, 0, 0, 0};
  {
    LAS char* tb = sm + B_UC + (chl * 2 + (e & 1)) * UC_S + 4 + (16 * (g >> 1) - 8 * (g & 1) + 8 + (e & ~1)) * 2 + mh * 256;
    LAS char* vb = sm + B_KA + chl * 4608 + 512 + (e - (g >> 1)) * 32 + (g & 1) * 16 - mh * 256 - 192;
    bf16x8 T[2][8], V[2][8];
    auto rdT = [&](int ee, int m) { LAS u32* tp = (LAS u32*)(tb + ee * 64 + m * 512); T[ee & 1][m] = as8((u32x4){tp[0], tp[1], tp[2], tp[3]}); };
    auto rdV = [&](int ee, int m) { V[ee & 1][m] = *(LAS bf16x8*)(vb + (3 - ee) * 64 + m * 512); };
#pragma unroll
    for (int m = 0; m < 8; ++m) { rdT(0, m); rdV(0, m); }
    SB();
#pragma unroll
    for (int ee = 0; ee < 4; ++ee) {
#pragma unroll
      for (int s = 0; s < 8; ++s) {
        if (ee < 3) { rdT(ee + 1, s); rdV(ee + 1, s); }
#pragma unroll
        for (int m = 0; m <= s; ++m) {
          if (m + s < 8) acc[m + s] = mfma32(V[ee & 1][s], T[ee & 1][m], acc[m + s]);
          if (m < s && m + s < 8) acc[m + s] = mfma32(V[ee & 1][m], T[ee & 1][s], acc[m + s]);
        }
        SB();
      }
    }
  }
  RT(5);
#pragma unroll
  for (int c = 0; c < 8; ++c) *(LAS f32x4*)(sm + B_SCR + (w * 8 + c) * 1024 + l * 16) = acc[c];
  LBAR();
#pragma unroll
  for (int k = 0; k < 2; ++k) {
    const int c = 2 * w + k;
    LAS f32x4* xp = (LAS f32x4*)(sm + B_SCR + c * 1024 + l * 16);
    const f32x4 y0 = xp[0] + xp[512], y1 = xp[1024] + xp[1536];
    *(u32x4*)(p.ws + WS_YT + (size_t)(((gch * 32 + 4 * c + g) * 32 + bi) * 256 + e * 16)) =
        (u32x4){pk2(y0[0], y1[0]), pk2(y0[1], y1[1]), pk2(y0[2], y1[2]), pk2(y0[3], y1[3])};
  }
  RT(6);
  RTDUMP(0);
}

__global__ __launch_bounds__(256, 2) void k_one(Params p) {
  __shared__ __attribute__((aligned(16))) char smem[81920];
  if (blockIdx.x < 256) roleB1(p, (LAS char*)smem, blockIdx.x); else roleA1(p, (LAS char*)smem, blockIdx.x - 256);
}

constexpr int C_WO = 0, C_KV = 65536, C_ZX = 81920, C_YT = 86016, C_XC = 95232, C_ST = 111616, C_END = C_ST + 4 * 2304;
__global__ __launch_bounds__(256, 2) void k_two(Params p) {
  __shared__ __attribute__((aligned(16))) char smem[C_END];
  LAS char* sm = (LAS char*)smem;
  const int a = blockIdx.x, xcd = a & 7, idx = a >> 3;
  const int rb = (xcd >> 1) * 8 + (idx & 7), cg = (xcd & 1) * 4 + (idx >> 3);
  const int t = threadIdx.x, w = rfl(t >> 6), l = t & 63, g = l >> 4, e = l & 15;
  const int sh = w & 1, kh = w >> 1;
  const u32 smb = (u32)(size_t)sm;
  RTDECL; RT(0);
  const rsrc_t rfin = mkrsrc(p.ws + WS_FIN, 16384), rwo = mkrsrc(p.wo, 512 * 1024);
  sfor<0, 4>([&](auto I) { dma16<I.v * 1024>(smb + C_KV + w * 4096, rfin, (u32)(l * 16), (u32)(w * 4096)); });
  const u32x2 zr = *(const u32x2*)(p.ws + WS_FINZ + (t >> 3) * 32 + (t & 3) * 8);
  u32x4 pq[16];
  {
    const char* pp_ = p.ws + WS_PQ + (size_t)((rb * 2 + sh) * 32 + kh * 16) * 1024 + l * 16;
#pragma unroll
    for (int i = 0; i < 16; ++i) pq[i] = *(const u32x4*)(pp_ + i * 1024);
  }
  const char* yt = p.ws + WS_YT + (size_t)(xcd * 32 + idx) * 8192 + t * 16;
  const u32x4 y0 = *(const u32x4*)yt, y1 = *(const u32x4*)(yt + 4096);
  const char* ut = (const char*)p.u + (size_t)(idx * 64 + (t >> 3)) * 1024 + xcd * 128 + (t & 7) * 16;
  const u32x4 u0 = *(const u32x4*)ut, u1 = *(const u32x4*)(ut + 32 * 1024);
  const u32x4 dk = *(const u32x4*)((const char*)p.dsk + xcd * 128 + (t & 7) * 16);
  SB();
  {
    const u32 vw = (u32)((l >> 3) * 1024 + (((l & 7) ^ (g << 1)) * 16));
    sfor<0, 16>([&](auto I) { constexpr int hp = (I.v >> 1) + (I.v & 1) * 8;
      dma16<0>(smb + C_WO + (hp * 4 + w) * 1024, rwo, vw, (u32)(cg * 128 + (hp * 32 + w * 8) * 1024)); });
  }
  SB();
  wait_vm<16>();
  asm volatile("" : "+v"(*(u32x2*)&zr));
  *(LAS u32x4*)(sm + C_ZX + t * 16) = (t & 4) ? (u32x4){0, 0, zr[0], zr[1]} : (u32x4){zr[0], zr[1], 0, 0};
#pragma unroll
  for (int r = 0; r < 4; ++r) {
    *(LAS u32*)(sm + C_YT + (16 * r + e) * 144 + (t >> 4) * 4) = y0[r];
    *(LAS u32*)(sm + C_YT + (16 * r + e) * 144 + ((t >> 4) + 16) * 4) = y1[r];
  }
  LBAR();
  RT(1);
  const f32x4 zero = {0, 0, 0, 0};
  u32x4 of[2][8];
#pragma unroll
  for (int j = 0; j < 8; ++j) {
    const u32x4 kf = *(LAS u32x4*)(sm + C_KV + (kh * 8 + j) * 1024 + l * 16);
#pragma unroll
    for (int hh = 0; hh < 2; ++hh) {
      const u32x4 U = pq[2 * j + hh];
      const bf16x8 zf = *(LAS bf16x8*)(sm + C_ZX + (kh * 16 + 2 * j + hh) * 128 + ((e >> 1) & 1) * 64 + g * 16);
      const bf16x4 kh4 = as4((u32x2){kf[2 * hh], kf[2 * hh + 1]});
      const f32x4 na = mfma16(kh4, as4((u32x2){U[0], U[1]}), zero);
      const f32x4 nb = mfma16(kh4, as4((u32x2){U[2], U[3]}), zero);
      const f32x4 dn = mfma32(zf, as8(U), zero);
      const float ra = __builtin_amdgcn_rcpf(dn[0] + 1e-6f), rb_ = __builtin_amdgcn_rcpf(dn[2] + 1e-6f);
      of[0][j][2 * hh] = pk2(na[0] * ra, na[1] * ra); of[0][j][2 * hh + 1] = pk2(na[2] * ra, na[3] * ra);
      of[1][j][2 * hh] = pk2(nb[0] * rb_, nb[1] * rb_); of[1][j][2 * hh + 1] = pk2(nb[2] * rb_, nb[3] * rb_);
    }
  }
#pragma unroll
  for (int k = 0; k < 2; ++k) {
    const u32x4 yv = *(LAS u32x4*)(sm + C_YT + ((t >> 3) + 32 * k) * 144 + (t & 7) * 16);
    const u32x4 uv = k ? u1 : u0;
    u32x4 o;
#pragma unroll
    for (int j = 0; j < 4; ++j) {
      const u32 sk = pk2(bf2f(uv[j]) * bf2f(dk[j]), bfhi(uv[j]) * bfhi(dk[j]));
      o[j] = pk2(bf2f(yv[j]) + bf2f(sk), bfhi(yv[j]) + bfhi(sk));
    }
    *(u32x4*)((char*)p.out + (size_t)(idx * 64 + (t >> 3) + 32 * k) * 2048 + 1024 + xcd * 128 + (t & 7) * 16) = o;
  }
  SB();
  RT(2);
  wait_vm<2>();
  BAR();
  RT(3);
  f32x4 acc[2][4];
#pragma unroll
  for (int c = 0; c < 4; ++c) { acc[0][c] = zero; acc[1][c] = zero; }
  const int q4 = e >> 2, pp = e & 3, fw = (2 * g + (q4 >> 1)) & 3;
  LAS char* wbase = sm + C_WO + kh * 32768 + (4 * g + q4) * 128 + pp * 8;
  bf16x8 wf[2][4];
  auto rdw = [&](int j, int c) { LAS char* wq = wbase + ((c ^ fw) * 32) + j * 4096; wf[j & 1][c] = cat8(trread(wq), trread(wq + 2048)); };
#pragma unroll
  for (int c = 0; c < 4; ++c) rdw(0, c);
  SB();
#pragma unroll
  for (int j = 0; j < 8; ++j) {
#pragma unroll
    for (int c = 0; c < 4; ++c) {
      if (j < 7) rdw(j + 1, c);
      acc[0][c] = mfma32(wf[j & 1][c], as8(of[0][j]), acc[0][c]);
      acc[1][c] = mfma32(wf[j & 1][c], as8(of[1][j]), acc[1][c]);
      SB();
    }
  }
  RT(4);
  f32x4 mine[4];
#pragma unroll
  for (int c = 0; c < 4; ++c) {
    *(LAS f32x4*)(sm + C_XC + (w * 4 + c) * 1024 + l * 16) = kh ? acc[0][c] : acc[1][c];
    mine[c] = kh ? acc[1][c] : acc[0][c];
  }
  LBAR();
  LAS char* st = sm + C_ST + w * 2304;
#pragma unroll
  for (int c = 0; c < 4; ++c) {
    const f32x4 s = mine[c] + *(LAS f32x4*)(sm + C_XC + ((w ^ 2) * 4 + c) * 1024 + l * 16);
    *(LAS u32x2*)(st + e * 144 + c * 32 + g * 8) = (u32x2){pk2(s[0], s[1]), pk2(s[2], s[3])};
  }
  WFENCE();
#pragma unroll
  for (int k = 0; k < 2; ++k) {
    const int row = (l >> 3) + 8 * k;
    *(u32x4*)((char*)p.out + (size_t)(rb * 64 + (sh * 2 + kh) * 16 + row) * 2048 + cg * 128 + (l & 7) * 16) = *(LAS u32x4*)(st + row * 144 + (l & 7) * 16);
  }
  RT(5);
  RTDUMP(1);
}

__global__ void k_diag2(char* ws) {
  const u64 tstart = __builtin_amdgcn_s_memrealtime();
  const u64* st = (const u64*)(ws + WS_DBG);
  u64 m = ~0ull;
  for (int i = threadIdx.x; i < 512 * 4; i += 64) { const u64 v = st[((size_t)(i / 4) * 8 + i % 4) * 16]; if (v <= tstart && tstart - v < 100000) m = v < m ? v : m; }
  for (int o = 32; o; o >>= 1) { u64 x = __shfl_xor(m, o); m = x < m ? x : m; }
  const u64 tt = *(const u64*)(ws + (30u << 20));
  const u64 t0 = m;
  const u64 q2 = 0;
  const u64 until = t0 + 10000 + q2 * 3000;
  while (__builtin_amdgcn_s_memrealtime() < until) __builtin_amdgcn_s_sleep(8);
}
extern "C" void kernel_launch(void* const* d_in, const int* in_sizes, int n_in, void* d_out, int out_size, void* d_ws, size_t ws_size,
                              hipStream_t stream) {
  Params p;
  p.x = (const u16*)d_in[0]; p.wqkv = (const u16*)d_in[1]; p.wo = (const u16*)d_in[2]; p.u = (const u16*)d_in[3];
  p.lar = (const u16*)d_in[4]; p.aim = (const u16*)d_in[5]; p.cr = (const u16*)d_in[6]; p.ci = (const u16*)d_in[7];
  p.ldt = (const u16*)d_in[8]; p.dsk = (const u16*)d_in[9]; p.out = (u16*)d_out; p.ws = (char*)d_ws;
  k_one<<<512, 256, 0, stream>>>(p);
  k_two<<<256, 256, 0, stream>>>(p);
  k_diag2<<<1, 64, 0, stream>>>(p.ws);
}
```

```cpp
#include <hip/hip_runtime.h>
#include <hip/hip_bf16.h>

typedef unsigned short u16;
typedef unsigned int u32;
typedef unsigned long long u64;
typedef short bf16x8 __attribute__((ext_vector_type(8)));
typedef short bf16x4 __attribute__((ext_vector_type(4)));
typedef float f32x4 __attribute__((ext_vector_type(4)));
typedef float f32x2 __attribute__((ext_vector_type(2)));
typedef u32 u32x4 __attribute__((ext_vector_type(4)));
typedef u32 u32x2 __attribute__((ext_vector_type(2)));
typedef __bf16 bfv2 __attribute__((ext_vector_type(2)));
typedef __amdgpu_buffer_rsrc_t rsrc_t;
#define GAS __attribute__((address_space(1)))
#define LAS __attribute__((address_space(3)))
#define DI __device__ __forceinline__
#define RLX_AGENT __ATOMIC_RELAXED, __HIP_MEMORY_SCOPE_AGENT
#define SB() __builtin_amdgcn_sched_barrier(0)

#define STAMP_AT(k, i) do {} while (0)
#define RTDECL do {} while (0)
#define RT(i) do {} while (0)
#define RTDUMP(k) do {} while (0)

DI float bf2f(u32 h) { return __uint_as_float(h << 16); }
DI float bfhi(u32 h) { return __uint_as_float(h & 0xffff0000u); }
DI u32 pk2(float lo, float hi) { bfv2 v = __builtin_convertvector((f32x2){lo, hi}, bfv2); return __builtin_bit_cast(u32, v); }
DI float rbf(float x) { return bf2f(pk2(x, x) & 0xffffu); }
DI f32x4 mfma32(bf16x8 a, bf16x8 b, f32x4 c) { return __builtin_amdgcn_mfma_f32_16x16x32_bf16(a, b, c, 0, 0, 0); }
DI f32x4 mfma16(bf16x4 a, bf16x4 b, f32x4 c) { return __builtin_amdgcn_mfma_f32_16x16x16bf16_1k(a, b, c, 0, 0, 0); }
DI bf16x8 as8(u32x4 v) { return __builtin_bit_cast(bf16x8, v); }
DI bf16x4 as4(u32x2 v) { return __builtin_bit_cast(bf16x4, v); }
DI u32 rfl(u32 v) { return __builtin_amdgcn_readfirstlane(v); }
DI rsrc_t mkrsrc(const void* base, u32 bytes) { return __builtin_amdgcn_make_buffer_rsrc((void*)base, 0, bytes, 0x00020000); }
template <int OFF> DI void dma16(u32 m0v, rsrc_t r, u32 voff, u32 soff) {
  asm volatile("s_mov_b32 m0, %0\n\ts_nop 0\n\tbuffer_load_dwordx4 %1, %2, %3 offen offset:%4 lds"
               :: "s"(m0v), "v"(voff), "s"(r), "s"(soff), "i"(OFF) : "memory");
}
template <int N> DI void wait_vm() { asm volatile("s_waitcnt vmcnt(%0)" :: "i"(N) : "memory"); }
DI void wait_lgkm0() { __builtin_amdgcn_s_waitcnt(0xC07F); asm volatile("" ::: "memory"); }
#define BAR() do { asm volatile("" ::: "memory"); __builtin_amdgcn_s_barrier(); asm volatile("" ::: "memory"); } while (0)
#define LBAR() do { wait_lgkm0(); BAR(); } while (0)
#define WFENCE() do { __builtin_amdgcn_wave_barrier(); asm volatile("" ::: "memory"); } while (0)
DI void st16_dev(rsrc_t r, u32 off, u32x4 v) { __builtin_amdgcn_raw_buffer_store_b128(v, r, off, 0, 16); }
DI u32x4 ld16_dev(rsrc_t r, u32 off) { return __builtin_amdgcn_raw_buffer_load_b128(r, off, 0, 16); }
DI u32x2 ld8_dev(rsrc_t r, u32 off) { return __builtin_amdgcn_raw_buffer_load_b64(r, off, 0, 16); }
DI bf16x4 trread(LAS char* a) { return __builtin_amdgcn_ds_read_tr16_b64_v4i16((LAS bf16x4*)a); }
DI bf16x8 cat8(bf16x4 a, bf16x4 b) { return __builtin_shufflevector(a, b, 0, 1, 2, 3, 4, 5, 6, 7); }
DI float phi(float t) { float e = __builtin_amdgcn_exp2f(t * 1.44269504088896f); return (t > 0.f ? t + 1.f : e) + 1e-6f; }
DI f32x2 cmul(f32x2 x, f32x2 w, f32x2 wn) {
  f32x2 t = x * (f32x2){w.x, w.x};
  return __builtin_elementwise_fma((f32x2){x.y, x.x}, wn, t);
}
template <int I> struct IC { static constexpr int v = I; };
template <int B, int E, class F> DI void sfor(F&& f) { if constexpr (B < E) { f(IC<B>{}); sfor<B + 1, E>(f); } }

constexpr size_t WS_EPOCH = 0;
constexpr size_t WS_PQF   = 4096;
constexpr size_t WS_FIN   = 16384;
constexpr u32 FIN_REP = 36864;
constexpr size_t WS_PART  = 512u << 10;
constexpr size_t WS_PQ    = 2u << 20;
constexpr size_t WS_GU    = 4u << 20;
constexpr size_t WS_GY    = 8u << 20;
constexpr size_t WS_DBG   = 16u << 20;
constexpr int SPIN_CAP = 4096;

struct Params {
  const u16 *x, *wqkv, *wo, *u, *lar, *aim, *cr, *ci, *ldt, *dsk;
  u16* out;
  char* ws;
};
#define SLP 4
template <int N, bool FIRST_DONE = false> DI void gather(rsrc_t r, u32 off, u32 stride, u32 tag, u32x4 (&v)[N]) {
  u32 pend = (1u << N) - 1u;
  if constexpr (!FIRST_DONE) {
#pragma unroll
    for (int i = 0; i < N; ++i) v[i] = ld16_dev(r, off + i * stride);
  }
#pragma unroll 1
  for (int spin = 0; spin < SPIN_CAP; ++spin) {
#pragma unroll
    for (int i = 0; i < N; ++i) if (__all((v[i][1] == tag) & (v[i][3] == tag))) pend &= ~(1u << i);
    if (!pend) break;
    __builtin_amdgcn_s_sleep(SLP);
    asm volatile("" ::: "memory");
#pragma unroll
    for (int i = 0; i < N; ++i) if ((pend >> i) & 1u) v[i] = ld16_dev(r, off + i * stride);
  }
}
DI u32 sload(const void* p) { return *(const __attribute__((address_space(4))) u32*)p; }

constexpr int A_W = 0, A_X = 49152;
constexpr int ndma(int s) { return (s >= -4 && s <= 14 ? 3 : 0) + (s >= -5 && s <= 10 ? 1 : 0); }
constexpr int A_KV = 65536, A_ZX = 0, A_XC0 = 4096, A_XC1 = 32768, NPRE = 3;
DI void roleA1(const Params& p, LAS char* sm, int a) {
  const int xcd = a & 7, idx = a >> 3, rbl = idx & 7, cgl = idx >> 3;
  const int rb = (xcd >> 1) * 8 + rbl, cg = (xcd & 1) * 4 + cgl;
  const int w = rfl(threadIdx.x >> 6), l = threadIdx.x & 63, g = l >> 4, e = l & 15;
  const int head = cg * 4 + w;
  const u32 smb = (u32)(size_t)sm;
  RTDECL; RT(0);
  __builtin_amdgcn_s_setprio(2);
  const rsrc_t rx = mkrsrc(p.x, 2048 * 1024), rw = mkrsrc(p.wqkv, 512 * 3072);
  const u32 vx = (u32)((l >> 3) * 1024 + (((l & 7) ^ (l >> 3)) * 16));
  const u32 vw = (u32)((l >> 3) * 3072 + (((l & 7) ^ (((g & 1) | ((w & 1) << 1)) << 1)) * 16));
  const int s0 = (rbl + 2 * cgl) & 7;
  const u32 sxb = (u32)((rb * 64 + w * 8) * 1024), swb = (u32)(w * 8 * 3072 + cg * 128);
  const rsrc_t rwo = mkrsrc(p.wo, 512 * 1024);
  const u32 vwo = (u32)((l >> 3) * 1024 + (((l & 7) ^ (g << 1)) * 16));
  auto dmaWo = [&](int hp) { dma16<0>(smb + (hp * 4 + w) * 1024, rwo, vwo, (u32)(cg * 128 + (hp * 32 + w * 8) * 1024)); };
  auto dmaW = [&](auto S, auto PT) {
    constexpr int j = S.v + 4, pt = PT.v;
    if constexpr (j >= 0 && j <= 15)
      dma16<pt * 1024>(smb + A_W + (j & 3) * 12288 + w * 3072, rw, vw, swb + (u32)((((s0 + (j >> 1)) & 7) * 2 + (j & 1)) * (32 * 3072)));
    else if constexpr (j > 15) dmaWo((j - 16) * 3 + pt);
  };
  auto dmaX = [&](auto S) {
    constexpr int s = S.v, m = (s + 5) >> 1, hf = (s + 5) & 1;
    if constexpr (s >= -5 && s <= 10)
      dma16<0>(smb + A_X + (m % 3) * 8192 + (hf * 4 + w) * 1024, rx, vx, sxb + (u32)(hf * 32 * 1024 + ((s0 + m) & 7) * 128));
  };
  sfor<-5, 0>([&](auto S) { dmaX(S); dmaW(S, IC<0>{}); dmaW(S, IC<1>{}); dmaW(S, IC<2>{}); });
  const u32 tag = sload(p.ws + WS_EPOCH) + 1u;
  const int xo = e * 128 + ((g ^ (l & 7)) * 16);
  const int q4 = e >> 2, pp = e & 3;
  const int wo_ = g * 3072 + q4 * 128 + ((w ^ (((q4 >> 1) & 1) | ((g & 1) << 1))) * 32) + pp * 8;
  LAS char* xp0 = sm + A_X + xo; LAS char* xp1 = sm + A_X + (xo ^ 64); LAS char* wp = sm + A_W + wo_;
  f32x4 aq[4], ak[4], av[4];
#pragma unroll
  for (int t = 0; t < 4; ++t) { aq[t] = (f32x4){0, 0, 0, 0}; ak[t] = aq[t]; av[t] = aq[t]; }
  bf16x8 xf[2][4], wf[2][3];
  auto rdX = [&](auto J, auto T) { constexpr int j = J.v, t = T.v;
    xf[j & 1][t] = *(LAS bf16x8*)(((j & 1) ? xp1 : xp0) + ((j >> 1) % 3) * 8192 + t * 2048); };
  auto rdW = [&](auto J, auto PT) { constexpr int j = J.v, pt = PT.v;
    wf[j & 1][pt] = cat8(trread(wp + (j & 3) * 12288 + pt * 1024), trread(wp + (j & 3) * 12288 + pt * 1024 + 512)); };
  wait_vm<ndma(-3) + ndma(-2) + ndma(-1)>();
  BAR();
  RT(5);
  sfor<0, 3>([&](auto PT) { rdW(IC<0>{}, PT); });
  sfor<0, 4>([&](auto T) { rdX(IC<0>{}, T); });
  sfor<0, 16>([&](auto J) {
    constexpr int j = J.v, c = j & 1;
    auto mm = [&](auto T) { constexpr int t = T.v;
      ak[t] = mfma32(xf[c][t], wf[c][1], ak[t]); av[t] = mfma32(xf[c][t], wf[c][2], av[t]); aq[t] = mfma32(wf[c][0], xf[c][t], aq[t]); };
    if constexpr (j < 15) {
      wait_vm<ndma(j - 2) + ndma(j - 1)>();
      wait_lgkm0();
      BAR();
      SB();
      rdW(IC<j + 1>{}, IC<1>{}); rdX(IC<j + 1>{}, IC<0>{}); rdW(IC<j + 1>{}, IC<2>{});
      dmaW(J, IC<0>{});
      SB();
      mm(IC<0>{});
      SB();
      rdW(IC<j + 1>{}, IC<0>{}); rdX(IC<j + 1>{}, IC<1>{});
      dmaW(J, IC<1>{});
      SB();
      mm(IC<1>{});
      SB();
      rdX(IC<j + 1>{}, IC<2>{}); rdX(IC<j + 1>{}, IC<3>{});
      dmaW(J, IC<2>{});
      SB();
      mm(IC<2>{});
      SB();
      dmaX(J);
      SB();
      mm(IC<3>{});
      SB();
    } else {
      LBAR();
      SB();
      sfor<0, 4>([&](auto T) { dmaWo(9 + 2 * T.v); if constexpr (T.v < 3) dmaWo(10 + 2 * T.v); SB(); mm(T); SB(); });
    }
  });
  RT(1);
  f32x4 kv = {0, 0, 0, 0}, zz = {0, 0, 0, 0};
  const u32x4 ones = {0x3f803f80u, 0x3f803f80u, 0x3f803f80u, 0x3f803f80u};
#pragma unroll
  for (int h = 0; h < 2; ++h) {
    u32x4 kf = {pk2(phi(ak[2 * h][0]), phi(ak[2 * h][1])), pk2(phi(ak[2 * h][2]), phi(ak[2 * h][3])),
                pk2(phi(ak[2 * h + 1][0]), phi(ak[2 * h + 1][1])), pk2(phi(ak[2 * h + 1][2]), phi(ak[2 * h + 1][3]))};
    u32x4 vf = {pk2(av[2 * h][0], av[2 * h][1]), pk2(av[2 * h][2], av[2 * h][3]),
                pk2(av[2 * h + 1][0], av[2 * h + 1][1]), pk2(av[2 * h + 1][2], av[2 * h + 1][3])};
    kv = mfma32(as8(kf), as8(vf), kv);
    zz = mfma32(as8(kf), as8(ones), zz);
  }
  const rsrc_t rpart = mkrsrc(p.ws + WS_PART, 32 * 9 * 4096);
  st16_dev(rpart, (u32)(((head * 9 + (l >> 3)) * 32 + rb) * 128 + (l & 7) * 16), (u32x4){pk2(kv[0], kv[1]), tag, pk2(kv[2], kv[3]), tag});
  if (e < 2) st16_dev(rpart, (u32)(((head * 9 + 8) * 32 + rb) * 128 + (e * 4 + g) * 16), (u32x4){pk2(zz[0], zz[1]), tag, pk2(zz[2], zz[3]), tag});
  SB();
  RT(2);
  const rsrc_t rpq = mkrsrc(p.ws + WS_PQ, 2u << 20);
#pragma unroll
  for (int h = 0; h < 2; ++h)
    st16_dev(rpq, (u32)(((rb * 2 + h) * 32 + head) * 1024 + l * 16),
             (u32x4){pk2(phi(aq[2 * h][0]), phi(aq[2 * h][1])), pk2(phi(aq[2 * h][2]), phi(aq[2 * h][3])),
                     pk2(phi(aq[2 * h + 1][0]), phi(aq[2 * h + 1][1])), pk2(phi(aq[2 * h + 1][2]), phi(aq[2 * h + 1][3]))});
  RT(3);
  const u64 ftag = ((u64)tag << 32) | (u32)~tag;
  wait_vm<0>();
  BAR();
  if (threadIdx.x == 192) __hip_atomic_store((GAS u64*)(p.ws + WS_PQF) + rb * 16 + cg, ftag, RLX_AGENT);
  const int sh = w & 1, kh = w >> 1;
  const int fw = (2 * g + (q4 >> 1)) & 3;
  LAS char* wbase = sm + kh * 32768 + (4 * g + q4) * 128 + pp * 8;
  bf16x8 wof[8][4];
  auto rdw = [&](int j, int c) { LAS char* wq = wbase + ((c ^ fw) * 32) + j * 4096; wof[j][c] = cat8(trread(wq), trread(wq + 2048)); };
#pragma unroll
  for (int j = 0; j < NPRE; ++j)
#pragma unroll
    for (int c = 0; c < 4; ++c) rdw(j, c);
  LBAR();
  RT(6);
  const rsrc_t rfin = mkrsrc(p.ws + WS_FIN, 8 * FIN_REP);
  if (w == 0 || (w == 1 && (a & 7) == ((a >> 3) & 7))) {
    const int rh = a >> 3, rt_ = w == 0 ? (a & 7) : 8;
    const u32 off = (u32)((rh * 9 + rt_) * 4096 + l * 16);
    u32x4 v[4];
    gather<4>(rpart, off, 1024, tag, v);
    float s[4] = {0.f, 0.f, 0.f, 0.f};
#pragma unroll
    for (int i = 0; i < 4; ++i) { s[0] += bf2f(v[i][0]); s[1] += bfhi(v[i][0]); s[2] += bf2f(v[i][2]); s[3] += bfhi(v[i][2]); }
#pragma unroll
    for (int j = 0; j < 4; ++j) { s[j] += __shfl_xor(s[j], 8); s[j] += __shfl_xor(s[j], 16); s[j] += __shfl_xor(s[j], 32); }
    st16_dev(rfin, (u32)((l >> 3) * FIN_REP + (rh >> 3) * 9216 + (w == 0 ? (rh & 7) * 1024 + rt_ * 128 : 8192 + (rh & 7) * 128) + (l & 7) * 16),
             (u32x4){pk2(s[0], s[1]), tag, pk2(s[2], s[3]), tag});
    RT(4);
  }
#pragma unroll 1
  for (int spin = 0; spin < SPIN_CAP; ++spin) {
    const u64 f = __hip_atomic_load((GAS u64*)(p.ws + WS_PQF) + rb * 16 + kh * 4 + (l & 3), RLX_AGENT);
    if (__all(f == ftag)) break;
    __builtin_amdgcn_s_sleep(SLP);
  }
  asm volatile("" ::: "memory");
  u32x4 pq[16];
#pragma unroll
  for (int i = 0; i < 16; ++i) pq[i] = ld16_dev(rpq, (u32)(((rb * 2 + sh) * 32 + kh * 16 + i) * 1024 + l * 16));
  wait_vm<0>();
  RT(7);
  BAR();
  RT(11);
  {
    u32x4 v[9];
    gather<9>(rfin, (u32)(xcd * FIN_REP + w * 9216 + l * 16), 1024, tag, v);
    const u32x4 vz = v[8];
#pragma unroll
    for (int i = 0; i < 4; ++i)
      *(LAS u32x4*)(sm + A_KV + (w * 4 + i) * 1024 + l * 16) = (u32x4){v[2 * i][0], v[2 * i][2], v[2 * i + 1][0], v[2 * i + 1][2]};
    *(LAS u32x4*)(sm + A_ZX + w * 1024 + l * 16) = (l & 4) ? (u32x4){0, 0, vz[0], vz[2]} : (u32x4){vz[0], vz[2], 0, 0};
  }
  LBAR();
  RT(8);
  if (a == 0 && threadIdx.x == 0) __hip_atomic_store((GAS u32*)(p.ws + WS_EPOCH), tag, RLX_AGENT);
  const f32x4 zero = {0, 0, 0, 0};
  f32x4 acc[2][4];
#pragma unroll
  for (int c = 0; c < 4; ++c) { acc[0][c] = zero; acc[1][c] = zero; }
  u32x4 kf[8]; bf16x8 zf[8][2];
  auto rdk = [&](int j) {
    kf[j] = *(LAS u32x4*)(sm + A_KV + (kh * 8 + j) * 1024 + l * 16);
#pragma unroll
    for (int hh = 0; hh < 2; ++hh) zf[j][hh] = *(LAS bf16x8*)(sm + A_ZX + (kh * 16 + 2 * j + hh) * 128 + ((e >> 1) & 1) * 64 + g * 16);
  };
  f32x4 na[8][2], nb[8][2], dn[8][2];
  u32x4 of[8][2];
  auto stA = [&](int j) {
#pragma unroll
    for (int hh = 0; hh < 2; ++hh) {
      const u32x4 U = pq[2 * j + hh];
      const bf16x4 k4 = as4((u32x2){kf[j][2 * hh], kf[j][2 * hh + 1]});
      dn[j][hh] = mfma32(zf[j][hh], as8(U), zero);
      na[j][hh] = mfma16(k4, as4((u32x2){U[0], U[1]}), zero);
      nb[j][hh] = mfma16(k4, as4((u32x2){U[2], U[3]}), zero);
    }
  };
  auto stV = [&](int j) {
#pragma unroll
    for (int hh = 0; hh < 2; ++hh) {
      const float ra = __builtin_amdgcn_rcpf(dn[j][hh][0] + 1e-6f), rb_ = __builtin_amdgcn_rcpf(dn[j][hh][2] + 1e-6f);
      of[j][0][2 * hh] = pk2(na[j][hh][0] * ra, na[j][hh][1] * ra); of[j][0][2 * hh + 1] = pk2(na[j][hh][2] * ra, na[j][hh][3] * ra);
      of[j][1][2 * hh] = pk2(nb[j][hh][0] * rb_, nb[j][hh][1] * rb_); of[j][1][2 * hh + 1] = pk2(nb[j][hh][2] * rb_, nb[j][hh][3] * rb_);
    }
  };
  auto stG = [&](int j) {
#pragma unroll
    for (int c = 0; c < 4; ++c) { acc[0][c] = mfma32(wof[j][c], as8(of[j][0]), acc[0][c]); acc[1][c] = mfma32(wof[j][c], as8(of[j][1]), acc[1][c]); }
  };
  rdk(0); rdk(1);
  SB();
  stA(0); rdk(2);
  SB();
#pragma unroll
  for (int j = 0; j <= 8; ++j) {
    if (j + 3 < 8) rdk(j + 3);
    if (j + 1 >= NPRE && j + 1 < 8) { rdw(j + 1, 0); rdw(j + 1, 1); rdw(j + 1, 2); rdw(j + 1, 3); }
    if (j + 1 < 8) stA(j + 1);
    if (j < 8) stV(j);
    if (j >= 1) stG(j - 1);
#pragma unroll
    for (int q = 0; q < 14; ++q) {
      __builtin_amdgcn_sched_group_barrier(0x008, 1, 0);
      __builtin_amdgcn_sched_group_barrier(0x100, 1, 0);
      __builtin_amdgcn_sched_group_barrier(0x002, 2, 0);
    }
    SB();
  }
  RT(9);
  f32x4 mine[4];
  LAS char* xc = sm + (kh ? A_XC1 : A_XC0) + sh * 4096 + l * 16;
  LAS char* xo_ = sm + (kh ? A_XC0 : A_XC1) + sh * 4096 + l * 16;
#pragma unroll
  for (int c = 0; c < 4; ++c) { *(LAS f32x4*)(xc + c * 1024) = kh ? acc[0][c] : acc[1][c]; mine[c] = kh ? acc[1][c] : acc[0][c]; }
  LBAR();
  LAS char* st = sm + A_KV + w * 2304;
#pragma unroll
  for (int c = 0; c < 4; ++c) {
    const f32x4 s = mine[c] + *(LAS f32x4*)(xo_ + c * 1024);
    *(LAS u32x2*)(st + e * 144 + c * 32 + g * 8) = (u32x2){pk2(s[0], s[1]), pk2(s[2], s[3])};
  }
  WFENCE();
#pragma unroll
  for (int k = 0; k < 2; ++k) {
    const int row = (l >> 3) + 8 * k;
    *(u32x4*)((char*)p.out + (size_t)(rb * 64 + (sh * 2 + kh) * 16 + row) * 2048 + cg * 128 + (l & 7) * 16) = *(LAS u32x4*)(st + row * 144 + (l & 7) * 16);
  }
  RT(10);
  RTDUMP(0);
}

constexpr int B_SCR = 0;
constexpr int B_UT = 37120;
constexpr int B_UC = 46336;
constexpr int UC_S = 4160;
constexpr int B_KA = 62976;
constexpr int B_PX = 72192;
static_assert(B_PX + 8192 <= 81920, "lds");
DI void roleB1(const Params& p, LAS char* sm, int b) {
  const int gch = b & 7, bi = b >> 3;
  const int t = threadIdx.x, w = rfl(t >> 6), l = t & 63, g = l >> 4, e = l & 15;
  const int chl = w >> 1, mh = w & 1, ch = gch * 64 + bi * 2 + chl;
  RTDECL; RT(0);
  const char* ut = (const char*)p.u + (size_t)(bi * 64 + (t >> 3)) * 1024 + gch * 128 + (t & 7) * 16;
  const u32x4 u0 = *(const u32x4*)ut, u1 = *(const u32x4*)(ut + 32 * 1024);
  const u32x4 dk = *(const u32x4*)((const char*)p.dsk + gch * 128 + (t & 7) * 16);
  const int pi = ch * 128 + mh * 64 + l;
  const u32 r_lar = p.lar[pi], r_aim = p.aim[pi], r_cr = p.cr[pi], r_ci = p.ci[pi], r_ldt = p.ldt[ch];
  const u32 tag = sload(p.ws + WS_EPOCH + (1 + gch) * 256) + 1u;
  if (t < 64) *(LAS u32x4*)(sm + B_KA + (t >> 5) * 4608 + (t & 31) * 16) = (u32x4){0, 0, 0, 0};
  else if (t < 96) *(LAS u32*)(sm + B_UC + ((t - 64) >> 3) * UC_S + (t & 7) * 4) = 0u;
  *(LAS u32x4*)(sm + B_UT + (t >> 3) * 144 + (t & 7) * 16) = u0;
  *(LAS u32x4*)(sm + B_UT + ((t >> 3) + 32) * 144 + (t & 7) * 16) = u1;
  asm volatile("" :: "s"(tag) : "memory");
  LBAR();
  const rsrc_t rgu = mkrsrc(p.ws + WS_GU, 512 * 8192);
  {
    const int cq = w * 4 + g;
    u32x2 r[4];
#pragma unroll
    for (int j = 0; j < 4; ++j) r[j] = *(LAS u32x2*)(sm + B_UT + (e * 4 + j) * 144 + cq * 8);
    const u32 off = (u32)((gch * 64 + cq * 4) * 8192 + (bi * 16 + e) * 16);
#pragma unroll
    for (int c = 0; c < 4; ++c) {
      const u32 sel = (c & 1) ? 0x07060302u : 0x05040100u;
      const u32 d0 = __builtin_amdgcn_perm(r[1][c >> 1], r[0][c >> 1], sel), d1 = __builtin_amdgcn_perm(r[3][c >> 1], r[2][c >> 1], sel);
      st16_dev(rgu, off + c * 8192, (u32x4){d0, tag, d1, tag});
    }
  }
  u32x4 sk0, sk1;
#pragma unroll
  for (int j = 0; j < 4; ++j) {
    sk0[j] = pk2(bf2f(u0[j]) * bf2f(dk[j]), bfhi(u0[j]) * bfhi(dk[j]));
    sk1[j] = pk2(bf2f(u1[j]) * bf2f(dk[j]), bfhi(u1[j]) * bfhi(dk[j]));
  }
  SB();
  RT(1);
  const float dt = rbf(expf(bf2f(r_ldt)));
  const float a2 = -rbf(expf(bf2f(r_lar))) * dt * 1.44269504088896f;
  const float th = bf2f(r_aim) * dt * 0.15915494309189535f;
  auto wpow = [&](float s) -> f32x2 {
    const float m = __builtin_amdgcn_exp2f(a2 * s), f = __builtin_amdgcn_fractf(th * s);
    return (f32x2){m * __builtin_amdgcn_cosf(f), m * __builtin_amdgcn_sinf(f)};
  };
  const f32x2 w1 = wpow(1.f), w8 = wpow(8.f), w128 = wpow(128.f);
  const f32x2 w1n = {-w1.y, w1.y}, w128n = {-w128.y, w128.y}, w8cn = {w8.y, -w8.y};
  LAS char* scr = sm + B_SCR + w * 9248;
  LAS u32* sw = (LAS u32*)(scr + l * 4);
  const int fo = e * 272 + g * 16;
  auto rdf = [&](int reg, int sh, int ks) { return *(LAS bf16x8*)(scr + reg * 4624 + fo + sh * 272 + ks * 64); };
  u32 pc[17], pr[16];
  f32x2 G[8];
  {
    f32x2 c = {1.f, 0.f};
    pc[0] = 0x3f80u;
    G[0] = (f32x2){2.f * bf2f(r_cr) * dt, 2.f * bf2f(r_ci) * dt};
    pr[0] = pk2(G[0].x, G[0].y);
#pragma unroll
    for (int i = 1; i <= 16; ++i) {
      c = cmul(c, w8, w8cn); pc[i] = pk2(c.x, c.y);
      if (i < 8) { G[i] = cmul(G[i - 1], w1, w1n); pr[i] = pk2(G[i].x, G[i].y); }
      else if (i < 16) { G[i - 8] = cmul(G[i - 8], w128, w128n); pr[i] = pk2(G[i - 8].x, G[i - 8].y); }
    }
  }
  SB();
#pragma unroll
  for (int i = 0; i <= 16; ++i) sw[i * 68] = pc[i];
#pragma unroll
  for (int i = 0; i < 16; ++i) sw[1156 + i * 68] = pr[i];
  WFENCE();
  bf16x8 cf[4], cf2[4], af[4];
#pragma unroll
  for (int ks = 0; ks < 4; ++ks) { cf[ks] = rdf(0, 0, ks); af[ks] = rdf(1, 0, ks); cf2[ks] = rdf(0, 1, ks); }
  WFENCE();
  SB();
  u32 ps[16], pt_[8];
#pragma unroll
  for (int h = 0; h < 2; ++h)
#pragma unroll
    for (int i = 0; i < 4; ++i) { G[2 * i] = cmul(G[2 * i], w128, w128n); ps[4 * h + i] = pk2(G[2 * i].x, G[2 * i].y); }
#pragma unroll
  for (int h = 0; h < 4; ++h)
#pragma unroll
    for (int i = 0; i < 2; ++i) { G[4 * i] = cmul(G[4 * i], w128, w128n); ps[8 + 2 * h + i] = pk2(G[4 * i].x, G[4 * i].y); }
#pragma unroll
  for (int h = 0; h < 8; ++h) { G[0] = cmul(G[0], w128, w128n); pt_[h] = pk2(G[0].x, G[0].y); }
  SB();
#pragma unroll
  for (int i = 0; i < 16; ++i) sw[i * 68] = ps[i];
#pragma unroll
  for (int h = 0; h < 8; ++h) sw[1156 + (4 * (h >> 1) + (h & 1)) * 68] = pt_[h];
  WFENCE();
  bf16x8 a1[4], a2f[4];
#pragma unroll
  for (int ks = 0; ks < 4; ++ks) { a1[ks] = rdf(0, 0, ks); a2f[ks] = rdf(1, 0, ks); }
  const u32 uoff = (u32)(ch * 8192 + (mh * 256 + l) * 16);
  u32x4 v[4], vp[1];
#pragma unroll
  for (int i = 0; i < 4; ++i) v[i] = ld16_dev(rgu, uoff + i * 1024);
  vp[0] = ld16_dev(rgu, (u32)(ch * 8192 + 255 * 16));
  SB();
  f32x4 d0 = {0, 0, 0, 0}, d1 = d0, d1s = d0, d2 = d0, d2s = d0;
#pragma unroll
  for (int ks = 0; ks < 4; ++ks) d0 = mfma32(af[ks], cf[ks], d0);
#pragma unroll
  for (int ks = 0; ks < 4; ++ks) {
    d1 = mfma32(a1[ks], cf[ks], d1); d1s = mfma32(a1[ks], cf2[ks], d1s);
    d2 = mfma32(a2f[ks], cf[ks], d2); d2s = mfma32(a2f[ks], cf2[ks], d2s);
  }
  {
    const f32x4 pa = {d2[0], d2[1], d2s[0], d2s[1]}, pb = {d1s[0], d1s[2], 0.f, 0.f};
    LAS f32x4* px = (LAS f32x4*)(sm + B_PX + w * 2048 + l * 16);
    px[0] = mh ? d0 : d1; px[64] = mh ? pa : pb;
    RT(2);
    LBAR();
    LAS f32x4* py = (LAS f32x4*)(sm + B_PX + (w ^ 1) * 2048 + l * 16);
    const f32x4 qa = py[0], qb = py[64];
    LAS char* ka = sm + B_KA + chl * 4608 + 512 + e * 16;
    if (mh == 0) {
      const f32x4 k0 = d0 + qa, k2 = pa + qb;
      *(LAS u32x2*)(ka + (g >> 1) * 256 + ((g & 1) ^ 1) * 8) = (u32x2){pk2(k0[3], k0[2]), pk2(k0[1], k0[0])};
#pragma unroll
      for (int r = 0; r < 2; ++r) {
        const float v_ = k2[r], w_ = k2[2 + r], m_ = (e & 1) ? w_ : v_;
        const u32 vv = pk2(v_, v_);
        *(LAS u32x4*)(ka + 2048 + (2 * g + r) * 256) = (u32x4){pk2(w_, w_), pk2(w_, m_), vv, vv};
      }
    } else {
      const f32x4 k1 = d1 + qa, k1s = pb + qb;
      const bool lo = g < 2;
      const float x_ = lo ? k1[2] : k1[1];
      *(LAS u32x4*)(ka + (lo ? 512 + g * 256 : 1024 + (g - 2) * 512)) =
          (u32x4){pk2(k1s[0], lo ? k1[3] : k1s[0]), pk2(x_, x_), pk2(x_, lo ? k1[1] : k1[0]), pk2(k1[0], k1[0])};
      if (!lo) *(LAS u32x4*)(ka + 1024 + (g - 2) * 512 + 256) = (u32x4){pk2(k1s[1], k1s[1]), pk2(k1[3], k1[3]), pk2(k1[3], k1[2]), pk2(k1[2], k1[2])};
    }
  }
  gather<4, true>(rgu, uoff, 1024, tag, v);
  gather<1, true>(rgu, (u32)(ch * 8192 + 255 * 16), 0, tag, vp);
  asm volatile("" :: "v"(vp[0]));
  RT(3);
  {
    LAS char* uc = sm + B_UC + chl * 2 * UC_S + 4 + 28 + (mh * 256 + l) * 8;
    u32 carry = mh ? vp[0][2] : 0u;
#pragma unroll
    for (int i = 0; i < 4; ++i) {
      const u32 prev = __builtin_amdgcn_update_dpp(carry, v[i][2], 0x138  , 0xf, 0xf, false);
      carry = __builtin_amdgcn_readlane(v[i][2], 63);
      *(LAS u32x2*)(uc + UC_S + i * 512) = (u32x2){v[i][0], v[i][2]};
      *(LAS u32x2*)(uc + i * 512) = (u32x2){__builtin_amdgcn_alignbit(v[i][0], prev, 16), __builtin_amdgcn_alignbit(v[i][2], v[i][0], 16)};
    }
  }
  LBAR();
  if (bi == 0 && t == 0) __hip_atomic_store((GAS u32*)(p.ws + WS_EPOCH + (1 + gch) * 256), tag, RLX_AGENT);
  RT(4);
  f32x4 acc[8];
#pragma unroll
  for (int c = 0; c < 8; ++c) acc[c] = (f32x4){0, 0, 0, 0};
  {
    LAS char* tb = sm + B_UC + (chl * 2 + (e & 1)) * UC_S + 4 + (16 * (g >> 1) - 8 * (g & 1) + 8 + (e & ~1)) * 2 + mh * 256;
    LAS char* vb = sm + B_KA + chl * 4608 + 512 + (e - (g >> 1)) * 32 + (g & 1) * 16 - mh * 256 - 192;
    bf16x8 T[2][8], V[2][8];
    auto rdT = [&](int ee, int m) { LAS u32* tp = (LAS u32*)(tb + ee * 64 + m * 512); T[ee & 1][m] = as8((u32x4){tp[0], tp[1], tp[2], tp[3]}); };
    auto rdV = [&](int ee, int m) { V[ee & 1][m] = *(LAS bf16x8*)(vb + (3 - ee) * 64 + m * 512); };
#pragma unroll
    for (int m = 0; m < 8; ++m) { rdT(0, m); rdV(0, m); }
    SB();
#pragma unroll
    for (int ee = 0; ee < 4; ++ee) {
#pragma unroll
      for (int s = 0; s < 8; ++s) {
        if (ee < 3) { rdT(ee + 1, s); rdV(ee + 1, s); }
#pragma unroll
        for (int m = 0; m <= s; ++m) {
          if (m + s < 8) acc[m + s] = mfma32(V[ee & 1][s], T[ee & 1][m], acc[m + s]);
          if (m < s && m + s < 8) acc[m + s] = mfma32(V[ee & 1][m], T[ee & 1][s], acc[m + s]);
        }
        SB();
      }
    }
  }
  RT(5);
#pragma unroll
  for (int c = 0; c < 8; ++c) *(LAS f32x4*)(sm + B_SCR + (w * 8 + c) * 1024 + l * 16) = acc[c];
  LBAR();
  const rsrc_t rgy = mkrsrc(p.ws + WS_GY, 4u << 20);
#pragma unroll
  for (int k = 0; k < 2; ++k) {
    const int c = 2 * w + k;
    LAS f32x4* xp = (LAS f32x4*)(sm + B_SCR + c * 1024 + l * 16);
    const f32x4 y0 = xp[0] + xp[512], y1 = xp[1024] + xp[1536];
    const u32 off = (u32)(((gch * 32 + 4 * c + g) * 32 + bi) * 512 + e * 16);
    st16_dev(rgy, off, (u32x4){pk2(y0[0], y1[0]), tag, pk2(y0[1], y1[1]), tag});
    st16_dev(rgy, off + 256, (u32x4){pk2(y0[2], y1[2]), tag, pk2(y0[3], y1[3]), tag});
  }
  RT(6);
  {
    const u32 off = (u32)((gch * 32 + bi) * 16384 + t * 16);
    wait_vm<0>();
    gather<4>(rgy, off, 4096, tag, v);
    RT(7);
    LAS char* yp = sm + B_UT + ((g & 1) * 32 + e) * 144 + (t >> 5) * 4;
#pragma unroll
    for (int i = 0; i < 4; ++i) { *(LAS u32*)(yp + i * 32) = v[i][0]; *(LAS u32*)(yp + 16 * 144 + i * 32) = v[i][2]; }
    LBAR();
#pragma unroll
    for (int k = 0; k < 2; ++k) {
      const u32x4 yv = *(LAS u32x4*)(sm + B_UT + ((t >> 3) + 32 * k) * 144 + (t & 7) * 16);
      const u32x4 sk = k ? sk1 : sk0;
      u32x4 o;
#pragma unroll
      for (int j = 0; j < 4; ++j) o[j] = pk2(bf2f(yv[j]) + bf2f(sk[j]), bfhi(yv[j]) + bfhi(sk[j]));
      *(u32x4*)((char*)p.out + (size_t)(bi * 64 + (t >> 3) + 32 * k) * 2048 + 1024 + gch * 128 + (t & 7) * 16) = o;
    }
  }
  RT(8);
  RTDUMP(0);
}

__global__ __launch_bounds__(256, 2) void k_one(Params p) {
  __shared__ __attribute__((aligned(16))) char smem[81920];
  if (blockIdx.x < 256) roleB1(p, (LAS char*)smem, blockIdx.x); else roleA1(p, (LAS char*)smem, blockIdx.x - 256);
}

extern "C" void kernel_launch(void* const* d_in, const int* in_sizes, int n_in, void* d_out, int out_size, void* d_ws, size_t ws_size,
                              hipStream_t stream) {
  Params p;
  p.x = (const u16*)d_in[0]; p.wqkv = (const u16*)d_in[1]; p.wo = (const u16*)d_in[2]; p.u = (const u16*)d_in[3];
  p.lar = (const u16*)d_in[4]; p.aim = (const u16*)d_in[5]; p.cr = (const u16*)d_in[6]; p.ci = (const u16*)d_in[7];
  p.ldt = (const u16*)d_in[8]; p.dsk = (const u16*)d_in[9]; p.out = (u16*)d_out; p.ws = (char*)d_ws;
  k_one<<<512, 256, 0, stream>>>(p);
}
```

```cpp
#include <hip/hip_runtime.h>
#include <hip/hip_bf16.h>

typedef unsigned short u16;
typedef unsigned int u32;
typedef unsigned long long u64;
typedef short bf16x8 __attribute__((ext_vector_type(8)));
typedef short bf16x4 __attribute__((ext_vector_type(4)));
typedef float f32x4 __attribute__((ext_vector_type(4)));
typedef float f32x2 __attribute__((ext_vector_type(2)));
typedef u32 u32x4 __attribute__((ext_vector_type(4)));
typedef u32 u32x2 __attribute__((ext_vector_type(2)));
typedef __bf16 bfv2 __attribute__((ext_vector_type(2)));
typedef __amdgpu_buffer_rsrc_t rsrc_t;
#define GAS __attribute__((address_space(1)))
#define LAS __attribute__((address_space(3)))
#define DI __device__ __forceinline__
#define RLX_AGENT __ATOMIC_RELAXED, __HIP_MEMORY_SCOPE_AGENT
#define SB() __builtin_amdgcn_sched_barrier(0)

#define STAMP_AT(k, i) do {} while (0)
#define RTDECL do {} while (0)
#define RT(i) do {} while (0)
#define RTDUMP(k) do {} while (0)

#if defined(__HIP_DEVICE_COMPILE__) && !defined(ALLOW_PK)
#pragma clang attribute push (__attribute__((target("no-packed-fp32-ops"))), apply_to = function)
#define NOPK_POP _Pragma("clang attribute pop")
#else
#define NOPK_POP
#endif
DI float asf(u32 h) { return __builtin_bit_cast(float, h); }
DI u32 asu(float f) { return __builtin_bit_cast(u32, f); }
DI float bf2f(u32 h) { return asf(h << 16); }
DI float bfhi(u32 h) { return asf(h & 0xffff0000u); }
DI bool wall(bool x) { return __builtin_amdgcn_ballot_w64(!x) == 0; }
DI float xsum_8_16_32(float s) {
  s += asf(__builtin_amdgcn_update_dpp(0u, asu(s), 0x128  , 0xf, 0xf, false));
  auto a = __builtin_amdgcn_permlane16_swap(asu(s), asu(s), false, false); s = asf(a[0]) + asf(a[1]);
  auto b = __builtin_amdgcn_permlane32_swap(asu(s), asu(s), false, false); return asf(b[0]) + asf(b[1]);
}
DI u32 pk2(float lo, float hi) { bfv2 v = __builtin_convertvector((f32x2){lo, hi}, bfv2); return __builtin_bit_cast(u32, v); }
DI float rbf(float x) { return bf2f(pk2(x, x) & 0xffffu); }
DI f32x4 mfma32(bf16x8 a, bf16x8 b, f32x4 c) { return __builtin_amdgcn_mfma_f32_16x16x32_bf16(a, b, c, 0, 0, 0); }
DI f32x4 mfma16(bf16x4 a, bf16x4 b, f32x4 c) { return __builtin_amdgcn_mfma_f32_16x16x16bf16_1k(a, b, c, 0, 0, 0); }
DI bf16x8 as8(u32x4 v) { return __builtin_bit_cast(bf16x8, v); }
DI bf16x4 as4(u32x2 v) { return __builtin_bit_cast(bf16x4, v); }
DI u32 rfl(u32 v) { return __builtin_amdgcn_readfirstlane(v); }
DI rsrc_t mkrsrc(const void* base, u32 bytes) { return __builtin_amdgcn_make_buffer_rsrc((void*)base, 0, bytes, 0x00020000); }
template <int OFF> DI void dma16(u32 m0v, rsrc_t r, u32 voff, u32 soff) {
  asm volatile("s_mov_b32 m0, %0\n\ts_nop 0\n\tbuffer_load_dwordx4 %1, %2, %3 offen offset:%4 lds"
               :: "s"(m0v), "v"(voff), "s"(r), "s"(soff), "i"(OFF) : "memory");
}
template <int N> DI void wait_vm() { asm volatile("s_waitcnt vmcnt(%0)" :: "i"(N) : "memory"); }
DI void wait_lgkm0() { __builtin_amdgcn_s_waitcnt(0xC07F); asm volatile("" ::: "memory"); }
#define BAR() do { asm volatile("" ::: "memory"); __builtin_amdgcn_s_barrier(); asm volatile("" ::: "memory"); } while (0)
#define LBAR() do { wait_lgkm0(); BAR(); } while (0)
#define WFENCE() do { __builtin_amdgcn_wave_barrier(); asm volatile("" ::: "memory"); } while (0)
DI void st16_dev(rsrc_t r, u32 off, u32x4 v) { __builtin_amdgcn_raw_buffer_store_b128(v, r, off, 0, 16); }
DI u32x4 ld16_dev(rsrc_t r, u32 off) { return __builtin_amdgcn_raw_buffer_load_b128(r, off, 0, 16); }
DI u32x2 ld8_dev(rsrc_t r, u32 off) { return __builtin_amdgcn_raw_buffer_load_b64(r, off, 0, 16); }
DI bf16x4 trread(LAS char* a) { return __builtin_amdgcn_ds_read_tr16_b64_v4i16((LAS bf16x4*)a); }
DI bf16x8 cat8(bf16x4 a, bf16x4 b) { return __builtin_shufflevector(a, b, 0, 1, 2, 3, 4, 5, 6, 7); }
DI float phi(float t) { float e = __builtin_amdgcn_exp2f(t * 1.44269504088896f); return (t > 0.f ? t + 1.f : e) + 1e-6f; }
struct cpx { float x, y; };
DI cpx cmul(cpx a, cpx w, float nwy) {
  return cpx{__builtin_fmaf(a.y, nwy, a.x * w.x), __builtin_fmaf(a.x, w.y, a.y * w.x)};
}
template <int I> struct IC { static constexpr int v = I; };
template <int B, int E, class F> DI void sfor(F&& f) { if constexpr (B < E) { f(IC<B>{}); sfor<B + 1, E>(f); } }

constexpr size_t WS_EPOCH = 0;
constexpr size_t WS_FIN   = 16384;
constexpr u32 FIN_REP = 36864;
constexpr size_t WS_PQF   = 384u << 10;
constexpr size_t WS_UF    = 512u << 10;
constexpr size_t WS_YF    = 640u << 10;
constexpr size_t WS_PART  = 768u << 10;
constexpr size_t WS_PQ    = 2u << 20;
constexpr size_t WS_GU    = 4u << 20;
constexpr size_t WS_DBG   = 16u << 20;
constexpr int SPIN_CAP = 4096;

struct Params {
  const u16 *x, *wqkv, *wo, *u, *lar, *aim, *cr, *ci, *ldt, *dsk;
  u16* out;
  char* ws;
};
#define YMODE 0
#define KW(d, v) d = v
#define PRIO_A 2
#define PRIO_B 0
#define PRIO_B2 0
#define NPUB 2
#define SLP 4
template <int N, bool FIRST_DONE = false> DI void gather(rsrc_t r, u32 off, u32 stride, u32 tag, u32x4 (&v)[N]) {
  u32 pend = (1u << N) - 1u;
  if constexpr (!FIRST_DONE) {
#pragma unroll
    for (int i = 0; i < N; ++i) v[i] = ld16_dev(r, off + i * stride);
  }
#pragma unroll 1
  for (int spin = 0; spin < SPIN_CAP; ++spin) {
#pragma unroll
    for (int i = 0; i < N; ++i) if (wall((v[i][1] == tag) & (v[i][3] == tag))) pend &= ~(1u << i);
    if (!pend) break;
    __builtin_amdgcn_s_sleep(SLP);
    asm volatile("" ::: "memory");
#pragma unroll
    for (int i = 0; i < N; ++i) if ((pend >> i) & 1u) v[i] = ld16_dev(r, off + i * stride);
  }
}
DI void wait_flags(GAS u64* f, u64 first, u64 want) {
#pragma unroll 1
  for (int spin = 0; spin < SPIN_CAP; ++spin) {
    if (wall(first == want)) break;
    __builtin_amdgcn_s_sleep(SLP);
    first = __hip_atomic_load(f, RLX_AGENT);
  }
  asm volatile("" ::: "memory");
}
DI u32 sload(const void* p) { return *(const __attribute__((address_space(4))) u32*)p; }

#define NPRE 6
#define ISSUED 6, 10, 13, 16, 19, 22, 24, 26, 27, 28
struct Sched { int issued[10]; };
constexpr Sched SC = {{ISSUED}};
constexpr int blk(int n) { return (n % 10) * 8192; }
constexpr bool sched_ok() { for (int m = -1; m < 8; ++m) if (SC.issued[m + 2] > 10 + 3 * (m + 1) || SC.issued[m + 2] - SC.issued[m + 1] > 4 || (m < 7 && SC.issued[m + 1] < 3 * m + 6)) return false; return true; }
static_assert(sched_ok() && SC.issued[9] >= 28 && SC.issued[9] <= 32, "schedule");
constexpr int A_KV0 = blk(30), A_KV1 = blk(31), A_Z = blk(32);
typedef float f32x16 __attribute__((ext_vector_type(16)));
DI f32x16 mfma3216(bf16x8 a, bf16x8 b, f32x16 c) { return __builtin_amdgcn_mfma_f32_32x32x16_bf16(a, b, c, 0, 0, 0); }

DI void roleA1(const Params& p, LAS char* sm, int a) {
  const int xcd = a & 7, idx = a >> 3;
  const int rb = xcd * 4 + (idx & 3), cg = idx >> 2;
  const int s0 = (2 * (idx & 3) + cg) & 7;
  const int w = rfl(threadIdx.x >> 6), l = threadIdx.x & 63, g = l >> 4, e = l & 15, hf = l >> 5;
  const int head = cg * 4 + w, sh = w & 1, kh = w >> 1;
  const u32 smb = (u32)(size_t)sm;
  RTDECL; RT(0);
  __builtin_amdgcn_s_setprio(PRIO_A);
  const rsrc_t rx = mkrsrc(p.x, 2048 * 1024), rw = mkrsrc(p.wqkv, 512 * 3072), rwo = mkrsrc(p.wo, 512 * 1024);
  const u32 vx = (u32)((l >> 3) * 1024 + (((l & 7) ^ (((w & 1) << 2) | g)) * 16));
  const u32 vw0 = (u32)((l >> 3) * 3072 + (((l & 7) ^ ((g & 1) << 1)) * 16)), vw1 = vw0 ^ 64u;
  const u32 vq = (u32)((l >> 3) * 3072 + (((l & 7) ^ ((g & 1) << 2)) * 16));
  const u32 vo = (u32)((l >> 3) * 1024 + (((l & 7) ^ ((g & 1) << 2)) * 16));
  const u32 sxb = (u32)((rb * 64 + w * 8) * 1024), swb = (u32)(w * 8 * 3072 + cg * 128), sob = (u32)(w * 8 * 1024 + cg * 128);
  auto dma = [&](auto N, auto H) {
    constexpr int n = N.v, h = H.v; constexpr u32 lb = blk(n);
    if constexpr (n < 24) {
      constexpr int m = n / 3, r = n % 3;
      if constexpr (r == 0) dma16<0>(smb + lb + (h * 4 + w) * 1024, rx, vx, sxb + (u32)(h * 32 * 1024 + ((s0 + m) & 7) * 128));
      else dma16<h * 1024>(smb + lb + w * 2048, rw, h ? vw1 : vw0, swb + 1024u + (u32)((((s0 + m) & 7) * 2 + (r - 1)) * (32 * 3072)));
    } else if constexpr (n < 32) dma16<0>(smb + lb + (h * 4 + w) * 1024, rw, vq, swb + (u32)((((s0 + n - 24) & 7) * 2 + h) * (32 * 3072)));
    else dma16<0>(smb + lb + (h * 4 + w) * 1024, rwo, vo, sob + (u32)(((n - 32) * 64 + h * 32) * 1024));
  };
  sfor<0, SC.issued[0]>([&](auto N) { dma(N, IC<0>{}); dma(N, IC<1>{}); });
  const u32 tag = sload(p.ws + WS_EPOCH) + 1u;
  const int q4 = e >> 2, pp = e & 3, qb = (q4 >> 1) & 1;
  LAS char* xp[2][4];
#pragma unroll
  for (int t = 0; t < 2; ++t)
#pragma unroll
    for (int ks = 0; ks < 4; ++ks) xp[t][ks] = sm + (t ^ sh) * 4096 + (l & 31) * 128 + (((2 * ks + hf) ^ ((l >> 1) & 7)) * 16);
  LAS char* wp = sm + hf * 2048 + (g & 1) * 1024 + q4 * 128 + ((w ^ (qb | ((g & 1) << 1))) * 32) + pp * 8;
  LAS char* qp = sm + hf * 1024 + q4 * 128 + (((2 * kh + (g & 1)) ^ (qb << 1)) * 32) + pp * 8;
  f32x16 ac[2];
#pragma unroll
  for (int r = 0; r < 16; ++r) { ac[0][r] = 0.f; ac[1][r] = 0.f; }
  bf16x8 xq[32], xf[2][4], wf[2][4];
  auto rdX = [&](auto M, auto KS, auto T) { constexpr int m = M.v, ks = KS.v, t = T.v;
    const bf16x8 v = *(LAS bf16x8*)(xp[t][ks] + blk(3 * m));
    if constexpr (t == 0) xq[4 * m + ks] = v; else xf[m & 1][ks] = v; };
  auto rdW = [&](auto M, auto KS) { constexpr int m = M.v, ks = KS.v; constexpr int o = blk(3 * m + 1 + ks / 2) + (ks & 1) * 4096;
    wf[m & 1][ks] = cat8(trread(wp + o), trread(wp + o + 512)); };
  auto issue = [&](auto M, auto K) {
    constexpr int n = SC.issued[M.v + 1] + K.v / 2;
    if constexpr (n < SC.issued[M.v + 2]) dma(IC<n>{}, IC<K.v & 1>{});
  };
  wait_vm<2 * (SC.issued[0] - 3)>();
  BAR();
  RT(5);
  sfor<0, 4>([&](auto KS) { rdW(IC<0>{}, KS); rdX(IC<0>{}, KS, IC<0>{}); rdX(IC<0>{}, KS, IC<1>{}); });
  SB();
  sfor<0, 8>([&](auto K) { issue(IC<-1>{}, K); });
  sfor<0, 8>([&](auto M) {
    constexpr int m = M.v, c = m & 1;
    if constexpr (m < 7) wait_vm<2 * (SC.issued[m + 1] - (3 * m + 6))>();
    wait_lgkm0();
    BAR();
    SB();
    sfor<0, 8>([&](auto I) {
      constexpr int i = I.v, ks = i / 2, t = i & 1;
      ac[t] = mfma3216(t == 0 ? xq[4 * m + ks] : xf[c][ks], wf[c][ks], ac[t]);
      SB();
      if constexpr (m < 7 && i < 6) {
        if constexpr (i < 4) { rdW(IC<m + 1>{}, I); rdX(IC<m + 1>{}, I, IC<0>{}); }
        else { rdX(IC<m + 1>{}, IC<2 * (i - 4)>{}, IC<1>{}); rdX(IC<m + 1>{}, IC<2 * (i - 4) + 1>{}, IC<1>{}); }
      }
      issue(M, I);
      SB();
    });
  });
  RT(1);
  f32x4 kv = {0, 0, 0, 0}, zz = {0, 0, 0, 0};
  const u32x4 ones = {0x3f803f80u, 0x3f803f80u, 0x3f803f80u, 0x3f803f80u};
  sfor<0, 2>([&](auto H) {
    constexpr int h = H.v;
    u32x4 kf, vf;
    sfor<0, 4>([&](auto I) {
      constexpr int i = I.v, r = 8 * h + 2 * i, n2 = 2 * SC.issued[9] + 4 * h + i;
      if constexpr (n2 < 64) { dma(IC<n2 / 2>{}, IC<n2 & 1>{}); SB(); }
      const auto s0_ = __builtin_amdgcn_permlane16_swap(asu(ac[0][r]), asu(ac[1][r]), false, false);
      const auto s1_ = __builtin_amdgcn_permlane16_swap(asu(ac[0][r + 1]), asu(ac[1][r + 1]), false, false);
      kf[i] = pk2(phi(asf(s0_[0])), phi(asf(s1_[0]))); vf[i] = pk2(asf(s0_[1]), asf(s1_[1]));
      SB();
    });
    kv = mfma32(as8(kf), as8(vf), kv);
    zz = mfma32(as8(kf), as8(ones), zz);
  });
  const rsrc_t rpart = mkrsrc(p.ws + WS_PART, 32 * 9 * 4096);
  st16_dev(rpart, (u32)(((head * 9 + (l >> 3)) * 32 + rb) * 128 + (l & 7) * 16), (u32x4){pk2(kv[0], kv[1]), tag, pk2(kv[2], kv[3]), tag});
  st16_dev(rpart, e < 2 ? (u32)(((head * 9 + 8) * 32 + rb) * 128 + (e * 4 + g) * 16) : 0x7ffffff0u, (u32x4){pk2(zz[0], zz[1]), tag, pk2(zz[2], zz[3]), tag});
  SB();
  RT(2);
  f32x16 aq;
#pragma unroll
  for (int r = 0; r < 16; ++r) aq[r] = 0.f;
  bf16x8 qf[4];
  auto rdQ = [&](auto KS) { constexpr int ks = KS.v; constexpr int o = blk(24 + ks / 4) + (ks & 3) * 2048;
    qf[ks & 3] = cat8(trread(qp + o), trread(qp + o + 512)); };
  sfor<0, 4>([&](auto I) { dma(IC<32 + I.v / 2>{}, IC<I.v & 1>{}); });
  wait_vm<6>();
  BAR();
  sfor<0, 3>(rdQ);
  SB();
  sfor<0, 32>([&](auto KS) {
    constexpr int ks = KS.v;
    if constexpr (ks == 16) BAR();
    aq = mfma3216(qf[ks & 3], xq[ks], aq);
    SB();
    if constexpr (ks + 3 < 32) rdQ(IC<ks + 3>{});
    if constexpr (ks >= 16 && (ks & 1) == 0) dma(IC<34 + (ks - 16) / 4>{}, IC<(ks / 2) & 1>{});
    SB();
  });
  BAR();
  sfor<0, 4>([&](auto I) { dma(IC<38 + I.v / 2>{}, IC<I.v & 1>{}); });
  RT(3);
  const u64 ftag = ((u64)tag << 32) | (u32)~tag;
  const bool isred = w == 0 || (w == 1 && (a & 7) == ((a >> 3) & 7));
  const int rh = a >> 3, rt_ = w == 0 ? (a & 7) : 8;
  const u32 roff = (u32)((rh * 9 + rt_) * 4096 + l * 16);
  u32x4 rv[4];
  if (isred) {
#pragma unroll
    for (int i = 0; i < 4; ++i) rv[i] = ld16_dev(rpart, roff + i * 1024);
  }
  SB();
  const rsrc_t rpq = mkrsrc(p.ws + WS_PQ, 2u << 20);
#pragma unroll
  for (int h = 0; h < 2; ++h)
    st16_dev(rpq, (u32)(((rb * 2 + sh) * 32 + cg * 4 + 2 * kh + h) * 1024 + l * 16),
             (u32x4){pk2(phi(aq[8 * h]), phi(aq[8 * h + 1])), pk2(phi(aq[8 * h + 2]), phi(aq[8 * h + 3])),
                     pk2(phi(aq[8 * h + 4]), phi(aq[8 * h + 5])), pk2(phi(aq[8 * h + 6]), phi(aq[8 * h + 7]))});
  const rsrc_t rfin = mkrsrc(p.ws + WS_FIN, 8 * FIN_REP);
  auto reduce = [&]() {
    float s[4] = {0.f, 0.f, 0.f, 0.f};
#pragma unroll
    for (int i = 0; i < 4; ++i) { s[0] += bf2f(rv[i][0]); s[1] += bfhi(rv[i][0]); s[2] += bf2f(rv[i][2]); s[3] += bfhi(rv[i][2]); }
#pragma unroll
    for (int j = 0; j < 4; ++j) s[j] = xsum_8_16_32(s[j]);
    st16_dev(rfin, (u32)((l >> 3) * FIN_REP + (rh >> 3) * 9216 + (w == 0 ? (rh & 7) * 1024 + rt_ * 128 : 8192 + (rh & 7) * 128) + (l & 7) * 16),
             (u32x4){pk2(s[0], s[1]), tag, pk2(s[2], s[3]), tag});
  };
  bool pending = isred;
  if (isred) {
    bool ok = true;
#pragma unroll
    for (int i = 0; i < 4; ++i) ok &= (rv[i][1] == tag) & (rv[i][3] == tag);
    if (wall(ok)) { reduce(); pending = false; RT(4); wait_vm<1>(); } else wait_vm<0>();
  } else wait_vm<0>();
  if (l == 0) __hip_atomic_store((GAS u64*)(p.ws + WS_PQF) + (((rb * 2 + sh) * 8 + cg) * 2 + kh) * 16, ftag, RLX_AGENT);
  BAR();
  u32 wob[2];
#pragma unroll
  for (int ct = 0; ct < 2; ++ct) wob[ct] = (u32)((4 * hf + q4) * 128 + (((2 * ct + (g & 1)) ^ (((q4 >> 1) & 1) << 1)) * 32) + pp * 8);
  bf16x8 wof[16][2];
  auto rdw = [&](auto H) {
    constexpr int h = H.v, b0 = blk(32 + (h >> 2)), b1 = blk(36 + (h >> 2));
    const int ub = kh ? b1 : b0;
#pragma unroll
    for (int ct = 0; ct < 2; ++ct) { LAS char* q = sm + ub + wob[ct] + (h & 3) * 2048; wof[h][ct] = cat8(trread(q), trread(q + 1024)); }
  };
  sfor<0, NPRE>(rdw);
  RT(6);
  if (pending) { gather<4, true>(rpart, roff, 1024, tag, rv); reduce(); RT(4); }
  LBAR();
  RT(11);
  GAS u64* flp = (GAS u64*)(p.ws + WS_PQF) + ((rb * 2 + sh) * 16 + kh * 8 + (l & 7)) * 16;
  u64 fl;
  {
    u32x4 v[9];
    gather<9>(rfin, (u32)(xcd * FIN_REP + w * 9216 + l * 16), 1024, tag, v);
    fl = __hip_atomic_load(flp, RLX_AGENT);
    LAS char* kt = sm + (kh ? A_KV1 : A_KV0) + sh * 4096 + (g & 1) * 256 + e * 16 + (g >> 1) * 8;
#pragma unroll
    for (int i = 0; i < 8; ++i) *(LAS u32x2*)(kt + i * 512) = (u32x2){v[i][0], v[i][2]};
    *(LAS u32x2*)(sm + A_Z + (w * 8 + (l >> 3)) * 32 + (l & 1) * 16 + ((l >> 1) & 1) * 8) = (u32x2){v[8][0], v[8][2]};
  }
  RT(7);
  wait_flags(flp, fl, ftag);
  u32x4 pq[16];
  const u32 pqoff = (u32)(((rb * 2 + sh) * 32 + kh * 16) * 1024 + l * 16);
#pragma unroll
  for (int i = 0; i < 16; ++i) pq[i] = ld16_dev(rpq, pqoff + i * 1024);
  LBAR();
  RT(8);
  if (a == 0 && threadIdx.x == 0) __hip_atomic_store((GAS u32*)(p.ws + WS_EPOCH), tag, RLX_AGENT);
  f32x16 acc[2], cinit;
#pragma unroll
  for (int r = 0; r < 16; ++r) { acc[0][r] = 0.f; acc[1][r] = 0.f; cinit[r] = r < 8 ? 0.f : 1e-6f; }
  const bool zrow = (l & 16) != 0;
  const u32 kbase = zrow ? (u32)(A_Z + kh * 512 + hf * 16) : (u32)((kh ? A_KV1 : A_KV0) + hf * 256 + e * 16);
  const u32 kstr = zrow ? 32u : 512u;
  bf16x8 kf[16]; f32x16 nd[16]; u32x4 of[16];
  auto rdk = [&](int h) { kf[h] = *(LAS bf16x8*)(sm + kbase + h * kstr); };
  auto stA = [&](int h) { nd[h] = mfma3216(kf[h], as8(pq[h]), cinit); };
  auto stV = [&](int h) {
    asm volatile("" :: "v"(nd[h]));
    const float r = __builtin_amdgcn_rcpf(nd[h][8]);
    of[h] = (u32x4){pk2(nd[h][0] * r, nd[h][1] * r), pk2(nd[h][2] * r, nd[h][3] * r), pk2(nd[h][4] * r, nd[h][5] * r), pk2(nd[h][6] * r, nd[h][7] * r)};
  };
  auto stG = [&](int h) { acc[0] = mfma3216(wof[h][0], as8(of[h]), acc[0]); acc[1] = mfma3216(wof[h][1], as8(of[h]), acc[1]); };
  rdk(0); rdk(1); rdk(2); rdk(3);
  SB();
  stA(0); stA(1);
  SB();
  sfor<0, 17>([&](auto H) {
    constexpr int h = H.v;
    if constexpr (h < 16) stV(h);
    if constexpr (h + 4 < 16) rdk(h + 4);
    if constexpr (h + 2 >= NPRE && h + 2 < 16) rdw(IC<h + 2>{});
    if constexpr (h + 2 < 16) stA(h + 2);
    if constexpr (h >= 1) stG(h - 1);
#pragma unroll
    for (int q = 0; q < 3; ++q) {
      __builtin_amdgcn_sched_group_barrier(0x008, 1, 0);
      __builtin_amdgcn_sched_group_barrier(0x100, 2, 0);
      __builtin_amdgcn_sched_group_barrier(0x402, 5, 0);
    }
    SB();
  });
  RT(9);
  BAR();
  {
    LAS char* xw = sm + kh * 17408 + (sh * 32 + (l & 31)) * 272 + hf * 16;
#pragma unroll
    for (int ct = 0; ct < 2; ++ct)
#pragma unroll
      for (int q = 0; q < 4; ++q)
        *(LAS f32x4*)(xw + ct * 128 + q * 32) = (f32x4){acc[ct][4 * q], acc[ct][4 * q + 1], acc[ct][4 * q + 2], acc[ct][4 * q + 3]};
  }
  LBAR();
#pragma unroll
  for (int k = 0; k < 2; ++k) {
    const int row = (threadIdx.x >> 3) + 32 * k;
    LAS char* xr = sm + row * 272 + (l & 7) * 32;
    const f32x4 s0_ = *(LAS f32x4*)xr + *(LAS f32x4*)(xr + 17408), s1_ = *(LAS f32x4*)(xr + 16) + *(LAS f32x4*)(xr + 17408 + 16);
    *(u32x4*)((char*)p.out + (size_t)(rb * 64 + row) * 2048 + cg * 128 + (l & 7) * 16) =
        (u32x4){pk2(s0_[0], s0_[1]), pk2(s0_[2], s0_[3]), pk2(s1_[0], s1_[1]), pk2(s1_[2], s1_[3])};
  }
  RT(10);
  RTDUMP(0);
}

constexpr int B_SCR = 0;
constexpr int B_UT = 37120;
constexpr int B_UC = 46336;
constexpr int UC_S = 4160;
constexpr int B_KA = 62976;
constexpr int B_PX = 72192;
static_assert(B_PX + 8192 <= 81920, "lds");
DI float exp_bf(float x) {
  const float t = x * 1.44268798828125f, r = x * 4.8884952e-06f;
  const float m = __builtin_amdgcn_exp2f(t);
  return __builtin_fmaf(m, r, m);
}
DI void roleB1(const Params& p, LAS char* sm, int b) {
  const int gch = b & 7, bi = b >> 3;
  const int t = threadIdx.x, w = rfl(t >> 6), l = t & 63, g = l >> 4, e = l & 15;
  const int chl = w >> 1, mh = w & 1, ch = gch * 64 + bi * 2 + chl;
  RTDECL; RT(0);
  __builtin_amdgcn_s_setprio(PRIO_B);
  const char* ut = (const char*)p.u + (size_t)(bi * 64 + (t >> 3)) * 1024 + gch * 128 + (t & 7) * 16;
  const u32x4 u0 = *(const u32x4*)ut, u1 = *(const u32x4*)(ut + 32 * 1024);
  const int pi = ch * 128 + mh * 64 + l;
  const u32 r_lar = p.lar[pi], r_aim = p.aim[pi], r_cr = p.cr[pi], r_ci = p.ci[pi], r_ldt = p.ldt[ch];
  const u32x4 dk = *(const u32x4*)((const char*)p.dsk + gch * 128 + (t & 7) * 16);
  const u32 tag = sload(p.ws + WS_EPOCH + (1 + gch) * 256) + 1u;
  u32 touch = 0;
  if (w == 3 && l < 21)
    touch = __hip_atomic_load((GAS u32*)(p.ws + (l < 12 ? WS_PQF + (size_t)(l >> 2) * (128u << 10) + (size_t)((gch * 32 + bi) * 4 + (l & 3)) * 128
                                                        : WS_FIN + (size_t)gch * FIN_REP + (bi * 9 + l - 12) * 128)), RLX_AGENT);
  if (t < 64) *(LAS u32x4*)(sm + B_KA + (t >> 5) * 4608 + (t & 31) * 16) = (u32x4){0, 0, 0, 0};
  else if (t < 96) *(LAS u32*)(sm + B_UC + ((t - 64) >> 3) * UC_S + (t & 7) * 4) = 0u;
  *(LAS u32x4*)(sm + B_UT + (t >> 3) * 144 + (t & 7) * 16) = u0;
  *(LAS u32x4*)(sm + B_UT + ((t >> 3) + 32) * 144 + (t & 7) * 16) = u1;
  asm volatile("" :: "s"(tag) : "memory");
  LBAR();
  const u64 ftag = ((u64)tag << 32) | (u32)~tag;
  const rsrc_t rgu = mkrsrc(p.ws + WS_GU, 2u << 20);
  {
    const int cq = w * 4 + g;
    u32x2 r[4];
#pragma unroll
    for (int j = 0; j < 4; ++j) r[j] = *(LAS u32x2*)(sm + B_UT + (e * 4 + j) * 144 + cq * 8);
    const u32 off = (u32)((gch * 32 + cq * 2) * 8192 + (bi * 16 + e) * 16);
#pragma unroll
    for (int c = 0; c < NPUB; ++c)
      st16_dev(rgu, off + c * 8192, (u32x4){__builtin_amdgcn_perm(r[1][c], r[0][c], 0x05040100u), __builtin_amdgcn_perm(r[3][c], r[2][c], 0x05040100u),
                                            __builtin_amdgcn_perm(r[1][c], r[0][c], 0x07060302u), __builtin_amdgcn_perm(r[3][c], r[2][c], 0x07060302u)});
  }
  SB();
  RT(1);
  const float dt = rbf(exp_bf(bf2f(r_ldt)));
  const float a2 = -rbf(exp_bf(bf2f(r_lar))) * dt * 1.44269504088896f;
  const float th = bf2f(r_aim) * dt * 0.15915494309189535f;
  auto wpow = [&](float s, float sgn) -> cpx {
    const float m = __builtin_amdgcn_exp2f(a2 * s), f = __builtin_amdgcn_fractf(th * s);
    return cpx{m * __builtin_amdgcn_cosf(f), sgn * m * __builtin_amdgcn_sinf(f)};
  };
  const cpx w1 = wpow(1.f, 1.f), w8c = wpow(8.f, -1.f), w128 = wpow(128.f, 1.f);
  float n1 = -w1.y, n8 = -w8c.y, n128 = -w128.y;
  asm volatile("" : "+v"(n1), "+v"(n8), "+v"(n128));
  LAS char* scr = sm + B_SCR + w * 9248;
  LAS u32* sw = (LAS u32*)(scr + l * 4);
  const int fo = e * 272 + g * 16;
  auto rdf = [&](int reg, int sh, int ks) { return *(LAS bf16x8*)(scr + reg * 4624 + fo + sh * 272 + ks * 64); };
#define KEEP4(a, b, c, d) asm volatile("" :: "v"(a), "v"(b), "v"(c), "v"(d))
  cpx G[8];
  u32 k0_ = 0, k1_ = 0, k2_ = 0, k3_ = 0;
  {
    cpx c = w8c;
    u32 pc = 0x3f80u, pr;
    G[0] = cpx{2.f * bf2f(r_cr) * dt, 2.f * bf2f(r_ci) * dt};
    pr = pk2(G[0].x, G[0].y);
#pragma unroll
    for (int i = 1; i <= 16; ++i) {
      if (i > 1) c = cmul(c, w8c, n8);
      const u32 nc = pk2(c.x, c.y);
      u32 nr = 0;
      if (i < 8) { G[i] = cmul(G[i - 1], w1, n1); nr = pk2(G[i].x, G[i].y); }
      else if (i < 16) { G[i - 8] = cmul(G[i - 8], w128, n128); nr = pk2(G[i - 8].x, G[i - 8].y); }
      if (i & 1) {
        KEEP4(k0_, k1_, k2_, k3_);
        sw[(i - 1) * 68] = pc; sw[i * 68] = nc; sw[1156 + (i - 1) * 68] = pr; sw[1156 + i * 68] = nr; SB();
        k0_ = pc; k1_ = nc; k2_ = pr; k3_ = nr;
      } else { pc = nc; pr = nr; }
    }
    sw[16 * 68] = pc;
  }
  WFENCE();
  bf16x8 cf[4], cf2[4], af[4];
#pragma unroll
  for (int ks = 0; ks < 4; ++ks) { cf[ks] = rdf(0, 0, ks); af[ks] = rdf(1, 0, ks); cf2[ks] = rdf(0, 1, ks); }
  WFENCE();
  if (NPUB) wait_vm<0>();
  if (NPUB && l == 0) __hip_atomic_store((GAS u64*)(p.ws + WS_UF) + ((gch * 4 + w) * 32 + bi) * 16, ftag, RLX_AGENT);
  asm volatile("" :: "v"(touch));
  u32x4 sk0, sk1;
#pragma unroll
  for (int j = 0; j < 4; ++j) {
    sk0[j] = pk2(bf2f(u0[j]) * bf2f(dk[j]), bfhi(u0[j]) * bfhi(dk[j]));
    sk1[j] = pk2(bf2f(u1[j]) * bf2f(dk[j]), bfhi(u1[j]) * bfhi(dk[j]));
  }
  SB();
#pragma unroll
  for (int q = 0; q < 6; ++q) {
    u32 o[4];
#pragma unroll
    for (int i = 0; i < 4; ++i) {
      const int gi = q < 2 ? 2 * i : q < 4 ? 4 * (i & 1) : 0;
      G[gi] = cmul(G[gi], w128, n128); o[i] = pk2(G[gi].x, G[gi].y);
    }
    KEEP4(k0_, k1_, k2_, k3_);
#pragma unroll
    for (int i = 0; i < 4; ++i) sw[q < 4 ? (4 * q + i) * 68 : 1156 + (8 * (q - 4) + 4 * (i >> 1) + (i & 1)) * 68] = o[i];
    SB();
    k0_ = o[0]; k1_ = o[1]; k2_ = o[2]; k3_ = o[3];
  }
  WFENCE();
  bf16x8 a1[4], a2f[4];
#pragma unroll
  for (int ks = 0; ks < 4; ++ks) { a1[ks] = rdf(0, 0, ks); a2f[ks] = rdf(1, 0, ks); }
  GAS u64* ufp = (GAS u64*)(p.ws + WS_UF) + ((gch * 4 + (bi >> 3)) * 32 + (l & 31)) * 16;
  u64 uf0 = ftag;
  if (NPUB && w == 0) uf0 = __hip_atomic_load(ufp, RLX_AGENT);
  SB();
  f32x4 d0 = {0, 0, 0, 0}, d1 = d0, d1s = d0, d2 = d0, d2s = d0;
#pragma unroll
  for (int ks = 0; ks < 4; ++ks) d0 = mfma32(af[ks], cf[ks], d0);
#pragma unroll
  for (int ks = 0; ks < 4; ++ks) {
    d1 = mfma32(a1[ks], cf[ks], d1); d1s = mfma32(a1[ks], cf2[ks], d1s);
    d2 = mfma32(a2f[ks], cf[ks], d2); d2s = mfma32(a2f[ks], cf2[ks], d2s);
  }
  u32x4 v[2], vp;
  {
    const f32x4 pa = {d2[0], d2[1], d2s[0], d2s[1]}, pb = {d1s[0], d1s[2], 0.f, 0.f};
    LAS f32x4* px = (LAS f32x4*)(sm + B_PX + w * 2048 + l * 16);
    px[0] = mh ? d0 : d1; px[64] = mh ? pa : pb;
    RT(2);
    wait_flags(ufp, uf0, ftag);
    LBAR();
    RT(9);
    const u32 uoff = (u32)((gch * 32 + bi) * 8192 + w * 2048);
    v[0] = ld16_dev(rgu, uoff + l * 16); v[1] = ld16_dev(rgu, uoff + 1024 + l * 16);
    vp = ld16_dev(rgu, w ? uoff - 16 : 0x7ffffff0u);
    if (bi == 0 && t == 0) __hip_atomic_store((GAS u32*)(p.ws + WS_EPOCH + (1 + gch) * 256), tag, RLX_AGENT);
    SB();
    LAS f32x4* py = (LAS f32x4*)(sm + B_PX + (w ^ 1) * 2048 + l * 16);
    const f32x4 qa = py[0], qb = py[64];
    LAS char* ka = sm + B_KA + chl * 4608 + 512 + e * 16;
    if (mh == 0) {
      const f32x4 k0 = d0 + qa, k2 = pa + qb;
      *(LAS u32x2*)(ka + (g >> 1) * 256 + ((g & 1) ^ 1) * 8) = (u32x2){pk2(k0[3], k0[2]), pk2(k0[1], k0[0])};
#pragma unroll
      for (int r = 0; r < 2; ++r) {
        const float v_ = k2[r], w_ = k2[2 + r], m_ = (e & 1) ? w_ : v_;
        const u32 vv = pk2(v_, v_);
        *(LAS u32x4*)(ka + 2048 + (2 * g + r) * 256) = (u32x4){pk2(w_, w_), pk2(w_, m_), vv, vv};
      }
    } else {
      const f32x4 k1 = d1 + qa, k1s = pb + qb;
      const bool lo = g < 2;
      const float x_ = lo ? k1[2] : k1[1];
      *(LAS u32x4*)(ka + (lo ? 512 + g * 256 : 1024 + (g - 2) * 512)) =
          (u32x4){pk2(k1s[0], lo ? k1[3] : k1s[0]), pk2(x_, x_), pk2(x_, lo ? k1[1] : k1[0]), pk2(k1[0], k1[0])};
      if (!lo) *(LAS u32x4*)(ka + 1024 + (g - 2) * 512 + 256) = (u32x4){pk2(k1s[1], k1s[1]), pk2(k1[3], k1[3]), pk2(k1[3], k1[2]), pk2(k1[2], k1[2])};
    }
  }
  asm volatile("" :: "v"(vp));
  RT(3);
#pragma unroll
  for (int c = 0; c < 2; ++c) {
    LAS char* uc = sm + B_UC + c * 2 * UC_S + 4 + 28 + (w * 128 + l) * 8;
    u32 carry = vp[2 * c + 1];
#pragma unroll
    for (int i = 0; i < 2; ++i) {
      const u32 lo = v[i][2 * c], hi = v[i][2 * c + 1];
      const u32 prev = __builtin_amdgcn_update_dpp(carry, hi, 0x138  , 0xf, 0xf, false);
      carry = __builtin_amdgcn_readlane(hi, 63);
      *(LAS u32x2*)(uc + UC_S + i * 512) = (u32x2){lo, hi};
      *(LAS u32x2*)(uc + i * 512) = (u32x2){__builtin_amdgcn_alignbit(lo, prev, 16), __builtin_amdgcn_alignbit(hi, lo, 16)};
    }
  }
  LBAR();
  RT(4);
  __builtin_amdgcn_s_setprio(PRIO_B2);
  f32x4 acc[8];
#pragma unroll
  for (int c = 0; c < 8; ++c) acc[c] = (f32x4){0, 0, 0, 0};
  {
    u32 tb[4];
#pragma unroll
    for (int k = 0; k < 4; ++k) {
      tb[k] = (u32)(size_t)sm + B_UC + (chl * 2 + (e & 1)) * UC_S + 4 + (16 * (g >> 1) - 8 * (g & 1) + 8 + (e & ~1)) * 2 + mh * 256 + k * 1024;
      asm volatile("" : "+v"(tb[k]));
    }
    u32 vb = (u32)(size_t)sm + B_KA + chl * 4608 + 512 + (e - (g >> 1)) * 32 + (g & 1) * 16 - mh * 256 - 192;
    asm volatile("" : "+v"(vb));
    bf16x8 T[2][8], V[2][8];
    auto rdT = [&](int ee, int m) { LAS u32* tp = (LAS u32*)(size_t)(tb[m >> 1] + ee * 64 + (m & 1) * 512); T[ee & 1][m] = as8((u32x4){tp[0], tp[1], tp[2], tp[3]}); };
    auto rdV = [&](int ee, int m) { V[ee & 1][m] = *(LAS bf16x8*)(size_t)(vb + (3 - ee) * 64 + m * 512); };
#pragma unroll
    for (int m = 0; m < 8; ++m) { rdT(0, m); rdV(0, m); SB(); }
#pragma unroll
    for (int ee = 0; ee < 4; ++ee) {
      if (ee < 3) {
#pragma unroll
        for (int s = 0; s < 8; ++s) { rdT(ee + 1, s); rdV(ee + 1, s); }
      }
#pragma unroll
      for (int s = 0; s < 8; ++s)
#pragma unroll
        for (int m = 0; m <= s; ++m) {
          if (m + s < 8) acc[m + s] = mfma32(V[ee & 1][s], T[ee & 1][m], acc[m + s]);
          if (m < s && m + s < 8) acc[m + s] = mfma32(V[ee & 1][m], T[ee & 1][s], acc[m + s]);
        }
      if (ee < 3) {
        __builtin_amdgcn_sched_group_barrier(0x008, 6, 0);
#pragma unroll
        for (int q = 0; q < 24; ++q) { __builtin_amdgcn_sched_group_barrier(0x008, 1, 0); __builtin_amdgcn_sched_group_barrier(0x100, 1, 0); }
        __builtin_amdgcn_sched_group_barrier(0x008, 6, 0);
      }
      SB();
    }
  }
  RT(5);
#pragma unroll
  for (int c = 0; c < 8; ++c) *(LAS f32x4*)(sm + B_SCR + (w * 8 + c) * 1024 + l * 16) = acc[c];
  LBAR();
  const rsrc_t rout = mkrsrc(p.out, 4u << 20);
#pragma unroll
  for (int k = 0; k < 2; ++k) {
    const int c = 2 * w + k;
    LAS f32x4* xp = (LAS f32x4*)(sm + B_SCR + c * 1024 + l * 16);
    const f32x4 y0 = xp[0] + xp[512], y1 = xp[1024] + xp[1536];
    st16_dev(rout, (u32)(((4 * c + g) * 64 + 2 * bi + (e >> 3)) * 2048 + 1024 + gch * 128 + (e & 7) * 16),
             (u32x4){pk2(y0[0], y1[0]), pk2(y0[1], y1[1]), pk2(y0[2], y1[2]), pk2(y0[3], y1[3])});
  }
  RT(6);
  wait_vm<0>();
  if (l == 0) __hip_atomic_store((GAS u64*)(p.ws + WS_YF) + ((gch * 4 + w) * 32 + bi) * 16, ftag, RLX_AGENT);
  if (w == 0) wait_flags((GAS u64*)(p.ws + WS_YF) + ((gch * 4 + (bi >> 3)) * 32 + (l & 31)) * 16, 0ull, ftag);
  BAR();
  RT(10);
  {
    const u32 off = (u32)((bi * 64 + (t >> 3)) * 2048 + 1024 + gch * 128 + (t & 7) * 16);
    v[0] = ld16_dev(rout, off); v[1] = ld16_dev(rout, off + 32 * 2048);
    LAS char* yp = sm + B_UT + e * 144 + (t >> 4) * 4;
#pragma unroll
    for (int i = 0; i < 2; ++i)
#pragma unroll
      for (int r = 0; r < 4; ++r) *(LAS u32*)(yp + r * 16 * 144 + i * 64) = v[i][r];
    RT(7);
    LBAR();
#pragma unroll
    for (int k = 0; k < 2; ++k) {
      const u32x4 yv = *(LAS u32x4*)(sm + B_UT + ((t >> 3) + 32 * k) * 144 + (t & 7) * 16);
      const u32x4 sk = k ? sk1 : sk0;
      u32x4 o;
#pragma unroll
      for (int j = 0; j < 4; ++j) o[j] = pk2(bf2f(yv[j]) + bf2f(sk[j]), bfhi(yv[j]) + bfhi(sk[j]));
      *(u32x4*)((char*)p.out + off + k * 32 * 2048) = o;
    }
  }
  RT(8);
  RTDUMP(0);
}

__global__ __launch_bounds__(256, 2) void k_one(Params p) {
  __shared__ __attribute__((aligned(16))) char smem[81920];
  if (blockIdx.x < 256) roleB1(p, (LAS char*)smem, blockIdx.x); else roleA1(p, (LAS char*)smem, blockIdx.x - 256);
}

NOPK_POP
extern "C" void kernel_launch(void* const* d_in, const int* in_sizes, int n_in, void* d_out, int out_size, void* d_ws, size_t ws_size,
                              hipStream_t stream) {
  Params p;
  p.x = (const u16*)d_in[0]; p.wqkv = (const u16*)d_in[1]; p.wo = (const u16*)d_in[2]; p.u = (const u16*)d_in[3];
  p.lar = (const u16*)d_in[4]; p.aim = (const u16*)d_in[5]; p.cr = (const u16*)d_in[6]; p.ci = (const u16*)d_in[7];
  p.ldt = (const u16*)d_in[8]; p.dsk = (const u16*)d_in[9]; p.out = (u16*)d_out; p.ws = (char*)d_ws;
  k_one<<<512, 256, 0, stream>>>(p);
}
```

```cpp
#include <hip/hip_runtime.h>
#include <hip/hip_bf16.h>

typedef unsigned short u16;
typedef unsigned int u32;
typedef unsigned long long u64;
typedef short bf16x8 __attribute__((ext_vector_type(8)));
typedef short bf16x4 __attribute__((ext_vector_type(4)));
typedef float f32x4 __attribute__((ext_vector_type(4)));
typedef float f32x2 __attribute__((ext_vector_type(2)));
typedef u32 u32x4 __attribute__((ext_vector_type(4)));
typedef u32 u32x2 __attribute__((ext_vector_type(2)));
typedef __bf16 bfv2 __attribute__((ext_vector_type(2)));
typedef __amdgpu_buffer_rsrc_t rsrc_t;
#define GAS __attribute__((address_space(1)))
#define LAS __attribute__((address_space(3)))
#define DI __device__ __forceinline__
#define RLX_AGENT __ATOMIC_RELAXED, __HIP_MEMORY_SCOPE_AGENT
#define SB() __builtin_amdgcn_sched_barrier(0)

#define STAMP_AT(k, i) do {} while (0)
#define RTDECL do {} while (0)
#define RT(i) do {} while (0)
#define RTDUMP(k) do {} while (0)

#if defined(__HIP_DEVICE_COMPILE__) && !defined(ALLOW_PK)
#pragma clang attribute push (__attribute__((target("no-packed-fp32-ops"))), apply_to = function)
#define NOPK_POP _Pragma("clang attribute pop")
#else
#define NOPK_POP
#endif
DI float asf(u32 h) { return __builtin_bit_cast(float, h); }
DI u32 asu(float f) { return __builtin_bit_cast(u32, f); }
DI float bf2f(u32 h) { return asf(h << 16); }
DI float bfhi(u32 h) { return asf(h & 0xffff0000u); }
DI bool wall(bool x) { return __builtin_amdgcn_ballot_w64(!x) == 0; }
DI float xsum_8_16_32(float s) {
  s += asf(__builtin_amdgcn_update_dpp(0u, asu(s), 0x128  , 0xf, 0xf, false));
  auto a = __builtin_amdgcn_permlane16_swap(asu(s), asu(s), false, false); s = asf(a[0]) + asf(a[1]);
  auto b = __builtin_amdgcn_permlane32_swap(asu(s), asu(s), false, false); return asf(b[0]) + asf(b[1]);
}
DI u32 pk2(float lo, float hi) { bfv2 v = __builtin_convertvector((f32x2){lo, hi}, bfv2); return __builtin_bit_cast(u32, v); }
DI float rbf(float x) { return bf2f(pk2(x, x) & 0xffffu); }
DI f32x4 mfma32(bf16x8 a, bf16x8 b, f32x4 c) { return __builtin_amdgcn_mfma_f32_16x16x32_bf16(a, b, c, 0, 0, 0); }
DI f32x4 mfma16(bf16x4 a, bf16x4 b, f32x4 c) { return __builtin_amdgcn_mfma_f32_16x16x16bf16_1k(a, b, c, 0, 0, 0); }
DI bf16x8 as8(u32x4 v) { return __builtin_bit_cast(bf16x8, v); }
DI bf16x4 as4(u32x2 v) { return __builtin_bit_cast(bf16x4, v); }
DI u32 rfl(u32 v) { return __builtin_amdgcn_readfirstlane(v); }
DI rsrc_t mkrsrc(const void* base, u32 bytes) { return __builtin_amdgcn_make_buffer_rsrc((void*)base, 0, bytes, 0x00020000); }
template <int OFF> DI void dma16(u32 m0v, rsrc_t r, u32 voff, u32 soff) {
  asm volatile("s_mov_b32 m0, %0\n\ts_nop 0\n\tbuffer_load_dwordx4 %1, %2, %3 offen offset:%4 lds"
               :: "s"(m0v), "v"(voff), "s"(r), "s"(soff), "i"(OFF) : "memory");
}
DI void dma4(u32 m0v, rsrc_t r, u32 voff) {
  asm volatile("s_mov_b32 m0, %0\n\ts_nop 0\n\tbuffer_load_dword %1, %2, 0 offen lds" :: "s"(m0v), "v"(voff), "s"(r) : "memory");
}
template <int N> DI void wait_vm() { asm volatile("s_waitcnt vmcnt(%0)" :: "i"(N) : "memory"); }
DI void wait_lgkm0() { __builtin_amdgcn_s_waitcnt(0xC07F); asm volatile("" ::: "memory"); }
#define BAR() do { asm volatile("" ::: "memory"); __builtin_amdgcn_s_barrier(); asm volatile("" ::: "memory"); } while (0)
#define LBAR() do { wait_lgkm0(); BAR(); } while (0)
#define WFENCE() do { __builtin_amdgcn_wave_barrier(); asm volatile("" ::: "memory"); } while (0)
DI void st16_dev(rsrc_t r, u32 off, u32x4 v) { __builtin_amdgcn_raw_buffer_store_b128(v, r, off, 0, 16); }
DI u32x4 ld16_dev(rsrc_t r, u32 off) { return __builtin_amdgcn_raw_buffer_load_b128(r, off, 0, 16); }
DI u32x2 ld8_dev(rsrc_t r, u32 off) { return __builtin_amdgcn_raw_buffer_load_b64(r, off, 0, 16); }
DI bf16x4 trread(LAS char* a) { return __builtin_amdgcn_ds_read_tr16_b64_v4i16((LAS bf16x4*)a); }
DI bf16x8 cat8(bf16x4 a, bf16x4 b) { return __builtin_shufflevector(a, b, 0, 1, 2, 3, 4, 5, 6, 7); }
DI float phi(float t) { float e = __builtin_amdgcn_exp2f(t * 1.44269504088896f); return (t > 0.f ? t + 1.f : e) + 1e-6f; }
struct cpx { float x, y; };
DI cpx cmul(cpx a, cpx w, float nwy) {
  return cpx{__builtin_fmaf(a.y, nwy, a.x * w.x), __builtin_fmaf(a.x, w.y, a.y * w.x)};
}
template <int I> struct IC { static constexpr int v = I; };
template <int B, int E, class F> DI void sfor(F&& f) { if constexpr (B < E) { f(IC<B>{}); sfor<B + 1, E>(f); } }

constexpr size_t WS_EPOCH = 0;
constexpr size_t WS_FIN   = 16384;
constexpr u32 FIN_REP = 36864;
constexpr size_t WS_PQF   = 384u << 10;
constexpr size_t WS_UF    = 512u << 10;
constexpr size_t WS_YF    = 640u << 10;
constexpr size_t WS_PART  = 768u << 10;
constexpr size_t WS_PQ    = 2u << 20;
constexpr size_t WS_GU    = 4u << 20;
constexpr size_t WS_DBG   = 16u << 20;
constexpr int SPIN_CAP = 4096;

struct Params {
  const u16 *x, *wqkv, *wo, *u, *lar, *aim, *cr, *ci, *ldt, *dsk;
  u16* out;
  char* ws;
};
#define YMODE 0
#define KW(d, v) d = v
#define PRIO_A 2
#define PRIO_B 0
#define PRIO_B1 3
#define PRIO_B2 0
#define NPUB 2
#define BDELAY 3300
#define SLP 4
template <int N, bool FIRST_DONE = false> DI void gather(rsrc_t r, u32 off, u32 stride, u32 tag, u32x4 (&v)[N]) {
  u32 pend = (1u << N) - 1u;
  if constexpr (!FIRST_DONE) {
#pragma unroll
    for (int i = 0; i < N; ++i) v[i] = ld16_dev(r, off + i * stride);
  }
#pragma unroll 1
  for (int spin = 0; spin < SPIN_CAP; ++spin) {
#pragma unroll
    for (int i = 0; i < N; ++i) if (wall((v[i][1] == tag) & (v[i][3] == tag))) pend &= ~(1u << i);
    if (!pend) break;
    __builtin_amdgcn_s_sleep(SLP);
    asm volatile("" ::: "memory");
#pragma unroll
    for (int i = 0; i < N; ++i) if ((pend >> i) & 1u) v[i] = ld16_dev(r, off + i * stride);
  }
}
DI void wait_flags(GAS u64* f, u64 first, u64 want) {
#pragma unroll 1
  for (int spin = 0; spin < SPIN_CAP; ++spin) {
    if (wall(first == want)) break;
    __builtin_amdgcn_s_sleep(SLP);
    first = __hip_atomic_load(f, RLX_AGENT);
  }
  asm volatile("" ::: "memory");
}
DI u32 sload(const void* p) { return *(const __attribute__((address_space(4))) u32*)p; }

#define KPS 6
#define UFSTEP 4
#define NPRE 6
#define ISSUED 6, 10, 13, 16, 19, 22, 24, 26, 27, 28
struct Sched { int issued[10]; };
constexpr Sched SC = {{ISSUED}};
constexpr int blk(int n) { return (n % 10) * 8192; }
constexpr bool sched_ok() { for (int m = -1; m < 8; ++m) if (SC.issued[m + 2] > 10 + 3 * (m + 1) || SC.issued[m + 2] - SC.issued[m + 1] > 4 || (m < 7 && SC.issued[m + 1] < 3 * m + 6)) return false; return true; }
static_assert(sched_ok() && SC.issued[9] >= 28 && SC.issued[9] <= 32, "schedule");
constexpr int A_KV0 = blk(30), A_KV1 = blk(31);
constexpr int BOFF = 81920;
constexpr int A_Z = BOFF + 72192, A_XC = BOFF;
constexpr int B_SCR = 0;
constexpr int B_UT = 37120;
constexpr int B_UC = 46336;
constexpr int UC_S = 4160;
constexpr int B_KA = 62976;
constexpr int B_PX = 72192;
constexpr int B_PAR = B_PX;
constexpr int B_JUNK = 81920 - 256;
static_assert(B_PX + 8192 <= B_JUNK, "lds");
typedef float f32x16 __attribute__((ext_vector_type(16)));
DI f32x16 mfma3216(bf16x8 a, bf16x8 b, f32x16 c) { return __builtin_amdgcn_mfma_f32_32x32x16_bf16(a, b, c, 0, 0, 0); }

DI void roleA1(const Params& p, LAS char* sm, int a) {
  const int xcd = a & 7, idx = a >> 3;
  const int rb = xcd * 4 + (idx & 3), cg = idx >> 2;
  const int t = threadIdx.x & 255, w = rfl(t >> 6), l = t & 63, g = l >> 4, e = l & 15, hf = l >> 5;
  const int head = cg * 4 + w, sh = w & 1, kh = w >> 1;
  RTDECL; RT(0);
  __builtin_amdgcn_s_setprio(PRIO_A);
  const u32 tag = sload(p.ws + WS_EPOCH) + 1u;
  u32 touch = 0;
  if (w == 3 && l < 21)
    touch = __hip_atomic_load((GAS u32*)(p.ws + (l < 12 ? WS_PQF + (size_t)(l >> 2) * (128u << 10) + (size_t)((xcd * 32 + idx) * 4 + (l & 3)) * 128
                                                        : WS_FIN + (size_t)xcd * FIN_REP + (idx * 9 + l - 12) * 128)), RLX_AGENT);
  if (w == 2 && l < 16) touch = *(const u32*)((const char*)p.wo + (size_t)(a * 16 + l) * 128);
  if (t < 64) *(LAS u32x4*)(sm + BOFF + B_KA + (t >> 5) * 4608 + (t & 31) * 16) = (u32x4){0, 0, 0, 0};
  else if (t < 96) *(LAS u32*)(sm + BOFF + B_UC + ((t - 64) >> 3) * UC_S + (t & 7) * 4) = 0u;
  const u64 ftag = ((u64)tag << 32) | (u32)~tag;
  const int q4 = e >> 2, pp = e & 3, qb = (q4 >> 1) & 1;
  LAS char* xp[2][4];
#pragma unroll
  for (int t = 0; t < 2; ++t)
#pragma unroll
    for (int ks = 0; ks < 4; ++ks) xp[t][ks] = sm + (t ^ sh) * 4096 + (l & 31) * 128 + (((2 * ks + hf) ^ ((l >> 1) & 7)) * 16);
  LAS char* wp = sm + hf * 2048 + (g & 1) * 1024 + q4 * 128 + ((w ^ (qb | ((g & 1) << 1))) * 32) + pp * 8;
  LAS char* qp = sm + hf * 1024 + q4 * 128 + (((2 * kh + (g & 1)) ^ (qb << 1)) * 32) + pp * 8;
  f32x16 ac[2];
#pragma unroll
  for (int r = 0; r < 16; ++r) { ac[0][r] = 0.f; ac[1][r] = 0.f; }
  bf16x8 xq[32], xf[2][4], wf[2][4];
  auto rdX = [&](auto M, auto KS, auto T) { constexpr int m = M.v, ks = KS.v, t = T.v;
    const bf16x8 v = *(LAS bf16x8*)(xp[t][ks] + blk(3 * m));
    if constexpr (t == 0) xq[4 * m + ks] = v; else xf[m & 1][ks] = v; };
  auto rdW = [&](auto M, auto KS) { constexpr int m = M.v, ks = KS.v; constexpr int o = blk(3 * m + 1 + ks / 2) + (ks & 1) * 4096;
    wf[m & 1][ks] = cat8(trread(wp + o), trread(wp + o + 512)); };
  LBAR();
  RT(5);
  sfor<0, 4>([&](auto KS) { rdW(IC<0>{}, KS); rdX(IC<0>{}, KS, IC<0>{}); rdX(IC<0>{}, KS, IC<1>{}); });
  {
    const rsrc_t rgu = mkrsrc(p.ws + WS_GU, 2u << 20);
    const int cq = w * 4 + g;
    u32x2 r[4];
#pragma unroll
    for (int j = 0; j < 4; ++j) r[j] = *(LAS u32x2*)(sm + BOFF + B_UT + (e * 4 + j) * 128 + (((cq >> 1) ^ (e & 7)) * 16) + (cq & 1) * 8);
    const u32 off = (u32)((xcd * 32 + cq * 2) * 8192 + (idx * 16 + e) * 16);
#pragma unroll
    for (int c = 0; c < 2; ++c)
      st16_dev(rgu, off + c * 8192, (u32x4){__builtin_amdgcn_perm(r[1][c], r[0][c], 0x05040100u), __builtin_amdgcn_perm(r[3][c], r[2][c], 0x05040100u),
                                            __builtin_amdgcn_perm(r[1][c], r[0][c], 0x07060302u), __builtin_amdgcn_perm(r[3][c], r[2][c], 0x07060302u)});
  }
  SB();
  sfor<0, 8>([&](auto M) {
    constexpr int m = M.v, c = m & 1;
    if constexpr (m == UFSTEP) {
      wait_vm<0>();
      if (l == 0) __hip_atomic_store((GAS u64*)(p.ws + WS_UF) + ((xcd * 4 + w) * 32 + idx) * 16, ftag, RLX_AGENT);
      asm volatile("" :: "v"(touch));
    }
    wait_lgkm0();
    BAR();
    SB();
    sfor<0, 8>([&](auto I) {
      constexpr int i = I.v, ks = i / 2, t = i & 1;
      ac[t] = mfma3216(t == 0 ? xq[4 * m + ks] : xf[c][ks], wf[c][ks], ac[t]);
      SB();
      if constexpr (m < 7 && i < 6) {
        if constexpr (i < 4) { rdW(IC<m + 1>{}, I); rdX(IC<m + 1>{}, I, IC<0>{}); }
        else { rdX(IC<m + 1>{}, IC<2 * (i - 4)>{}, IC<1>{}); rdX(IC<m + 1>{}, IC<2 * (i - 4) + 1>{}, IC<1>{}); }
      }
      SB();
    });
  });
  RT(1);
  f32x4 kv = {0, 0, 0, 0}, zz = {0, 0, 0, 0};
  const u32x4 ones = {0x3f803f80u, 0x3f803f80u, 0x3f803f80u, 0x3f803f80u};
#pragma unroll
  for (int h = 0; h < 2; ++h) {
    u32x4 kf, vf;
#pragma unroll
    for (int i = 0; i < 4; ++i) {
      const int r = 8 * h + 2 * i;
      const auto s0_ = __builtin_amdgcn_permlane16_swap(asu(ac[0][r]), asu(ac[1][r]), false, false);
      const auto s1_ = __builtin_amdgcn_permlane16_swap(asu(ac[0][r + 1]), asu(ac[1][r + 1]), false, false);
      kf[i] = pk2(phi(asf(s0_[0])), phi(asf(s1_[0]))); vf[i] = pk2(asf(s0_[1]), asf(s1_[1]));
    }
    kv = mfma32(as8(kf), as8(vf), kv);
    zz = mfma32(as8(kf), as8(ones), zz);
  }
  const rsrc_t rpart = mkrsrc(p.ws + WS_PART, 32 * 9 * 4096);
  st16_dev(rpart, (u32)(((head * 9 + (l >> 3)) * 32 + rb) * 128 + (l & 7) * 16), (u32x4){pk2(kv[0], kv[1]), tag, pk2(kv[2], kv[3]), tag});
  st16_dev(rpart, e < 2 ? (u32)(((head * 9 + 8) * 32 + rb) * 128 + (e * 4 + g) * 16) : 0x7ffffff0u, (u32x4){pk2(zz[0], zz[1]), tag, pk2(zz[2], zz[3]), tag});
  SB();
  RT(2);
  f32x16 aq;
#pragma unroll
  for (int r = 0; r < 16; ++r) aq[r] = 0.f;
  bf16x8 qf[4];
  auto rdQ = [&](auto KS) { constexpr int ks = KS.v; constexpr int o = blk(24 + ks / 4) + (ks & 3) * 2048;
    qf[ks & 3] = cat8(trread(qp + o), trread(qp + o + 512)); };
  BAR();
  sfor<0, 3>(rdQ);
  SB();
  sfor<0, 32>([&](auto KS) {
    constexpr int ks = KS.v;
    aq = mfma3216(qf[ks & 3], xq[ks], aq);
    SB();
    if constexpr (ks + 3 < 32) rdQ(IC<ks + 3>{});
    SB();
  });
  const bool isred = w == 0 || (w == 1 && (a & 7) == ((a >> 3) & 7));
  const int rh = a >> 3, rt_ = w == 0 ? (a & 7) : 8;
  const u32 roff = (u32)((rh * 9 + rt_) * 4096 + l * 16);
  u32x4 rv[4];
  if (isred) {
#pragma unroll
    for (int i = 0; i < 4; ++i) rv[i] = ld16_dev(rpart, roff + i * 1024);
  }
  BAR();
  RT(3);
  const rsrc_t rpq = mkrsrc(p.ws + WS_PQ, 2u << 20);
#pragma unroll
  for (int h = 0; h < 2; ++h)
    st16_dev(rpq, (u32)(((rb * 2 + sh) * 32 + cg * 4 + 2 * kh + h) * 1024 + l * 16),
             (u32x4){pk2(phi(aq[8 * h]), phi(aq[8 * h + 1])), pk2(phi(aq[8 * h + 2]), phi(aq[8 * h + 3])),
                     pk2(phi(aq[8 * h + 4]), phi(aq[8 * h + 5])), pk2(phi(aq[8 * h + 6]), phi(aq[8 * h + 7]))});
  BAR();
  const rsrc_t rfin = mkrsrc(p.ws + WS_FIN, 8 * FIN_REP);
  auto reduce = [&]() {
    float s[4] = {0.f, 0.f, 0.f, 0.f};
#pragma unroll
    for (int i = 0; i < 4; ++i) { s[0] += bf2f(rv[i][0]); s[1] += bfhi(rv[i][0]); s[2] += bf2f(rv[i][2]); s[3] += bfhi(rv[i][2]); }
#pragma unroll
    for (int j = 0; j < 4; ++j) s[j] = xsum_8_16_32(s[j]);
    st16_dev(rfin, (u32)((l >> 3) * FIN_REP + (rh >> 3) * 9216 + (w == 0 ? (rh & 7) * 1024 + rt_ * 128 : 8192 + (rh & 7) * 128) + (l & 7) * 16),
             (u32x4){pk2(s[0], s[1]), tag, pk2(s[2], s[3]), tag});
  };
  bool pending = isred;
  if (isred) {
    bool ok = true;
#pragma unroll
    for (int i = 0; i < 4; ++i) ok &= (rv[i][1] == tag) & (rv[i][3] == tag);
    if (wall(ok)) { reduce(); pending = false; RT(4); wait_vm<1>(); } else wait_vm<0>();
  } else wait_vm<0>();
  if (l == 0) __hip_atomic_store((GAS u64*)(p.ws + WS_PQF) + (((rb * 2 + sh) * 8 + cg) * 2 + kh) * 16, ftag, RLX_AGENT);
  RT(6);
  if (pending) { gather<4, true>(rpart, roff, 1024, tag, rv); reduce(); RT(4); }
  wait_flags((GAS u64*)(p.ws + WS_PQF) + ((rb * 2 + sh) * 16 + kh * 8 + (l & 7)) * 16, 0ull, ftag);
  RT(11);
  u32x4 pq[16];
  const u32 pqoff = (u32)(((rb * 2 + sh) * 32 + kh * 16) * 1024 + l * 16);
#pragma unroll
  for (int i = 0; i < 16; ++i) pq[i] = ld16_dev(rpq, pqoff + i * 1024);
  u32x4 fv[9];
  const u32 foff = (u32)(xcd * FIN_REP + w * 9216 + l * 16);
#pragma unroll
  for (int i = 0; i < 9; ++i) fv[i] = ld16_dev(rfin, foff + i * 1024);
  BAR();
  u32 wob[2];
#pragma unroll
  for (int ct = 0; ct < 2; ++ct) wob[ct] = (u32)((4 * hf + q4) * 128 + (((2 * ct + (g & 1)) ^ (((q4 >> 1) & 1) << 1)) * 32) + pp * 8);
  bf16x8 wof[16][2];
  auto rdw = [&](auto H) {
    constexpr int h = H.v, b0 = blk(32 + (h >> 2)), b1 = blk(36 + (h >> 2));
    const int ub = kh ? b1 : b0;
#pragma unroll
    for (int ct = 0; ct < 2; ++ct) { LAS char* q = sm + ub + wob[ct] + (h & 3) * 2048; wof[h][ct] = cat8(trread(q), trread(q + 1024)); }
  };
  sfor<0, NPRE>(rdw);
  {
    gather<9, true>(rfin, foff, 1024, tag, fv);
    RT(7);
    LAS char* kt = sm + (kh ? A_KV1 : A_KV0) + sh * 4096 + (g & 1) * 256 + e * 16 + (g >> 1) * 8;
#pragma unroll
    for (int i = 0; i < 8; ++i) *(LAS u32x2*)(kt + i * 512) = (u32x2){fv[i][0], fv[i][2]};
    *(LAS u32x2*)(sm + A_Z + (w * 8 + (l >> 3)) * 32 + (l & 1) * 16 + ((l >> 1) & 1) * 8) = (u32x2){fv[8][0], fv[8][2]};
  }
  LBAR();
  RT(8);
  if (a == 0 && t == 0) __hip_atomic_store((GAS u32*)(p.ws + WS_EPOCH), tag, RLX_AGENT);
  f32x16 acc[2], cinit;
#pragma unroll
  for (int r = 0; r < 16; ++r) { acc[0][r] = 0.f; acc[1][r] = 0.f; cinit[r] = r < 8 ? 0.f : 1e-6f; }
  const bool zrow = (l & 16) != 0;
  const u32 kbase = zrow ? (u32)(A_Z + kh * 512 + hf * 16) : (u32)((kh ? A_KV1 : A_KV0) + hf * 256 + e * 16);
  const u32 kstr = zrow ? 32u : 512u;
  bf16x8 kf[16]; f32x16 nd[16]; u32x4 of[16];
  auto rdk = [&](int h) { kf[h] = *(LAS bf16x8*)(sm + kbase + h * kstr); };
  auto stA = [&](int h) { nd[h] = mfma3216(kf[h], as8(pq[h]), cinit); };
  auto stV = [&](int h) {
    asm volatile("" :: "v"(nd[h]));
    const float r = __builtin_amdgcn_rcpf(nd[h][8]);
    of[h] = (u32x4){pk2(nd[h][0] * r, nd[h][1] * r), pk2(nd[h][2] * r, nd[h][3] * r), pk2(nd[h][4] * r, nd[h][5] * r), pk2(nd[h][6] * r, nd[h][7] * r)};
  };
  auto stG = [&](int h) { acc[0] = mfma3216(wof[h][0], as8(of[h]), acc[0]); acc[1] = mfma3216(wof[h][1], as8(of[h]), acc[1]); };
  rdk(0); rdk(1); rdk(2); rdk(3);
  SB();
  stA(0); stA(1);
  SB();
  sfor<0, 17>([&](auto H) {
    constexpr int h = H.v;
    if constexpr (h < 16) stV(h);
    if constexpr (h + 4 < 16) rdk(h + 4);
    if constexpr (h + 2 >= NPRE && h + 2 < 16) rdw(IC<h + 2>{});
    if constexpr (h + 2 < 16) stA(h + 2);
    if constexpr (h >= 1) stG(h - 1);
#pragma unroll
    for (int q = 0; q < 3; ++q) {
      __builtin_amdgcn_sched_group_barrier(0x008, 1, 0);
      __builtin_amdgcn_sched_group_barrier(0x100, 2, 0);
      __builtin_amdgcn_sched_group_barrier(0x402, 5, 0);
    }
    SB();
  });
  RT(9);
  {
    LAS char* xw = sm + A_XC + kh * 17408 + (sh * 32 + (l & 31)) * 272 + hf * 16;
#pragma unroll
    for (int ct = 0; ct < 2; ++ct)
#pragma unroll
      for (int q = 0; q < 4; ++q)
        *(LAS f32x4*)(xw + ct * 128 + q * 32) = (f32x4){acc[ct][4 * q], acc[ct][4 * q + 1], acc[ct][4 * q + 2], acc[ct][4 * q + 3]};
  }
  LBAR();
#pragma unroll
  for (int k = 0; k < 2; ++k) {
    const int row = (t >> 3) + 32 * k;
    LAS char* xr = sm + A_XC + row * 272 + (l & 7) * 32;
    const f32x4 s0_ = *(LAS f32x4*)xr + *(LAS f32x4*)(xr + 17408), s1_ = *(LAS f32x4*)(xr + 16) + *(LAS f32x4*)(xr + 17408 + 16);
    *(u32x4*)((char*)p.out + (size_t)(rb * 64 + row) * 2048 + cg * 128 + (l & 7) * 16) =
        (u32x4){pk2(s0_[0], s0_[1]), pk2(s0_[2], s0_[3]), pk2(s1_[0], s1_[1]), pk2(s1_[2], s1_[3])};
  }
  RT(10);
  RTDUMP(0);
  asm volatile(".Lk_code_end_a:");
}

struct ConvPairs { int t[36], v[36]; };
constexpr ConvPairs mkpairs() {
  ConvPairs c{}; int k = 0;
  for (int s = 0; s < 8; ++s) {
    for (int m = 0; m <= s; ++m) if (m + s < 8) { c.t[k] = m; c.v[k] = s; ++k; }
    for (int m = 0; m < s; ++m) if (m + s < 8) { c.t[k] = s; c.v[k] = m; ++k; }
  }
  return c;
}
constexpr ConvPairs CP = mkpairs();
DI float exp_bf(float x) {
  const float t = x * 1.44268798828125f, r = x * 4.8884952e-06f;
  const float m = __builtin_amdgcn_exp2f(t);
  return __builtin_fmaf(m, r, m);
}
DI void roleB1(const Params& p, LAS char* sm, int b) {
  const int gch = b & 7, bi = b >> 3;
  const int t = threadIdx.x, w = rfl(t >> 6), l = t & 63, g = l >> 4, e = l & 15;
  const int chl = w >> 1, mh = w & 1;
  RTDECL; RT(0);
  __builtin_amdgcn_s_setprio(PRIO_B);
  const u32 smB = (u32)(size_t)sm, smA = smB - BOFF;
  const int rb = gch * 4 + (bi & 3), cg = bi >> 2, s0 = (2 * (bi & 3) + cg) & 7;
  const rsrc_t rx = mkrsrc(p.x, 2048 * 1024), rw = mkrsrc(p.wqkv, 512 * 3072), rwo = mkrsrc(p.wo, 512 * 1024);
  const u32 vx = (u32)((l >> 3) * 1024 + (((l & 7) ^ (((w & 1) << 2) | g)) * 16));
  const u32 vw0 = (u32)((l >> 3) * 3072 + (((l & 7) ^ ((g & 1) << 1)) * 16)), vw1 = vw0 ^ 64u;
  const u32 vq = (u32)((l >> 3) * 3072 + (((l & 7) ^ ((g & 1) << 2)) * 16));
  const u32 vo = (u32)((l >> 3) * 1024 + (((l & 7) ^ ((g & 1) << 2)) * 16));
  const u32 sxb = (u32)((rb * 64 + w * 8) * 1024), swb = (u32)(w * 8 * 3072 + cg * 128), sob = (u32)(w * 8 * 1024 + cg * 128);
  auto dma = [&](auto N, auto H) {
    constexpr int n = N.v, h = H.v; constexpr u32 lb = blk(n);
    if constexpr (n < 24) {
      constexpr int m = n / 3, r = n % 3;
      if constexpr (r == 0) dma16<0>(smA + lb + (h * 4 + w) * 1024, rx, vx, sxb + (u32)(h * 32 * 1024 + ((s0 + m) & 7) * 128));
      else dma16<h * 1024>(smA + lb + w * 2048, rw, h ? vw1 : vw0, swb + 1024u + (u32)((((s0 + m) & 7) * 2 + (r - 1)) * (32 * 3072)));
    } else if constexpr (n < 32) dma16<0>(smA + lb + (h * 4 + w) * 1024, rw, vq, swb + (u32)((((s0 + n - 24) & 7) * 2 + h) * (32 * 3072)));
    else dma16<0>(smA + lb + (h * 4 + w) * 1024, rwo, vo, sob + (u32)(((n - 32) * 64 + h * 32) * 1024));
  };
  sfor<0, 3>([&](auto N) { dma(N, IC<0>{}); dma(N, IC<1>{}); });
  {
    const u16* pa = w == 0 ? p.lar : w == 1 ? p.aim : w == 2 ? p.cr : p.ci;
    dma16<0>(smB + B_PAR + w * 1024, mkrsrc(pa, 512 * 256), l < 32 ? (u32)(l * 16) : 0x7ffffff0u, (u32)((gch * 64 + bi * 2) * 256));
    dma16<0>(smB + B_PAR + 4096, mkrsrc(p.dsk, 1024), l < 8 ? (u32)(l * 16) : 0x7ffffff0u, (u32)(gch * 128));
    const rsrc_t ru = mkrsrc(p.u, 2048 * 1024);
    const u32 vu = (u32)((l >> 3) * 1024 + (((l & 7) ^ ((2 * w + (l >> 5)) & 7)) * 16));
    dma16<0>(smB + B_UT + w * 1024, ru, vu, (u32)((bi * 64 + w * 8) * 1024 + gch * 128));
    dma16<0>(smB + B_UT + (4 + w) * 1024, ru, vu, (u32)((bi * 64 + 32 + w * 8) * 1024 + gch * 128));
  }
  sfor<3, SC.issued[0]>([&](auto N) { dma(N, IC<0>{}); dma(N, IC<1>{}); });
  const u32 tag = sload(p.ws + WS_EPOCH) + 1u;
  const u32 ldt2 = sload(p.ldt + gch * 64 + bi * 2);
  const u64 ftag = ((u64)tag << 32) | (u32)~tag;
  asm volatile("" :: "s"(tag) : "memory");
  u32 r_lar, r_aim, r_cr, r_ci;
  float dt, a2, th, n1, n8, n128;
  cpx w1, w8c, w128, cc, G[8];
  u32 pc[17], pr[16], ps[24];
  u32x4 sk[2], dk;
  auto wpow = [&](float s, float sgn) -> cpx {
    const float m = __builtin_amdgcn_exp2f(a2 * s), f = __builtin_amdgcn_fractf(th * s);
    return cpx{m * __builtin_amdgcn_cosf(f), sgn * m * __builtin_amdgcn_sinf(f)};
  };
  auto kst = [&](auto I) {
    constexpr int i = I.v;
    if constexpr (i == 0) {
      LAS u16* q = (LAS u16*)(sm + B_PAR) + w * 64 + l;
      r_lar = q[0]; r_aim = q[512]; r_cr = q[1024]; r_ci = q[1536];
      dt = rbf(exp_bf(chl ? bfhi(ldt2) : bf2f(ldt2)));
    } else if constexpr (i == 1) {
      a2 = -rbf(exp_bf(bf2f(r_lar))) * dt * 1.44269504088896f;
      th = bf2f(r_aim) * dt * 0.15915494309189535f;
      G[0] = cpx{2.f * bf2f(r_cr) * dt, 2.f * bf2f(r_ci) * dt};
      pc[0] = 0x3f80u; pr[0] = pk2(G[0].x, G[0].y);
      asm volatile("" : "+v"(a2), "+v"(th), "+v"(pr[0]));
    } else if constexpr (i == 2) {
      w1 = wpow(1.f, 1.f); w8c = wpow(8.f, -1.f);
      n1 = -w1.y; n8 = -w8c.y;
      asm volatile("" : "+v"(n1), "+v"(n8), "+v"(w1.x), "+v"(w1.y), "+v"(w8c.x), "+v"(w8c.y));
    } else if constexpr (i == 3) {
      w128 = wpow(128.f, 1.f); n128 = -w128.y;
      asm volatile("" : "+v"(n128), "+v"(w128.x), "+v"(w128.y));
    } else if constexpr (i < 20) {
      constexpr int j = i - 3;
      if constexpr (j == 1) cc = w8c; else cc = cmul(cc, w8c, n8);
      pc[j] = pk2(cc.x, cc.y);
      asm volatile("" : "+v"(pc[j]));
      if constexpr (j < 8) { G[j] = cmul(G[j - 1], w1, n1); pr[j] = pk2(G[j].x, G[j].y); }
      else if constexpr (j < 16) { G[j - 8] = cmul(G[j - 8], w128, n128); pr[j] = pk2(G[j - 8].x, G[j - 8].y); }
      if constexpr (j < 16) asm volatile("" : "+v"(pr[j]));
    } else if constexpr (i < 32) {
#pragma unroll
      for (int k = 2 * (i - 20); k < 2 * (i - 20) + 2; ++k) {
        const int gi = k < 8 ? 2 * (k & 3) : k < 16 ? 4 * (k & 1) : 0;
        G[gi] = cmul(G[gi], w128, n128); ps[k] = pk2(G[gi].x, G[gi].y);
        asm volatile("" : "+v"(ps[k]));
      }
    } else if constexpr (i == 32) {
      dk = *(LAS u32x4*)(sm + B_PAR + 4096 + (t & 7) * 16);
#pragma unroll
      for (int k = 0; k < 2; ++k) sk[k] = *(LAS u32x4*)(sm + B_UT + ((t >> 3) + 32 * k) * 128 + (((t & 7) ^ ((t >> 5) & 7)) * 16));
    } else if constexpr (i < 35) {
      constexpr int k = i - 33;
#pragma unroll
      for (int j = 0; j < 4; ++j) sk[k][j] = pk2(bf2f(sk[k][j]) * bf2f(dk[j]), bfhi(sk[k][j]) * bfhi(dk[j]));
      asm volatile("" : "+v"(sk[k]));
    }
  };
  wait_vm<2 * (SC.issued[0] - 3)>();
  BAR();
  RT(1);
  sfor<-1, 8>([&](auto M) {
    constexpr int m = M.v;
    if constexpr (m >= 0) {
      if constexpr (m < 7) wait_vm<2 * (SC.issued[m + 1] - (3 * m + 6))>();
      BAR();
    }
    SB();
    constexpr int last = m < 7 ? SC.issued[m + 2] : 32;
    sfor<0, 12>([&](auto K) {
      constexpr int n = SC.issued[m + 1] + K.v / 2;
      if constexpr (n < last) { dma(IC<n>{}, IC<K.v & 1>{}); SB(); }
      if constexpr (K.v < KPS) { kst(IC<(m + 1) * KPS + K.v>{}); SB(); }
    });
  });
  static_assert(8 * KPS >= 35 && 32 - SC.issued[8] <= 6, "all pieces placed");
  RT(11);
  const rsrc_t rgu = mkrsrc(p.ws + WS_GU, 2u << 20);
  LAS char* scr = sm + B_SCR + w * 9248;
  LAS u32* sw = (LAS u32*)(scr + l * 4);
  const int fo = e * 272 + g * 16;
  auto rdf = [&](int reg, int sh, int ks) { return *(LAS bf16x8*)(scr + reg * 4624 + fo + sh * 272 + ks * 64); };
  GAS u64* ufp = (GAS u64*)(p.ws + WS_UF) + ((gch * 4 + (bi >> 3)) * 32 + ((l < 8 || w == 0) ? 8 * w + (l & 7) : 8 * w - 1)) * 16;
  const u64 uf0 = __hip_atomic_load(ufp, RLX_AGENT);
  wait_vm<1>();
  BAR();
  __builtin_amdgcn_s_setprio(PRIO_B1);
  SB();
#pragma unroll
  for (int i = 0; i <= 16; ++i) sw[i * 68] = pc[i];
#pragma unroll
  for (int i = 0; i < 16; ++i) sw[1156 + i * 68] = pr[i];
  WFENCE();
  bf16x8 cf[4], cf2[4], af[4];
#pragma unroll
  for (int ks = 0; ks < 4; ++ks) { cf[ks] = rdf(0, 0, ks); af[ks] = rdf(1, 0, ks); cf2[ks] = rdf(0, 1, ks); }
  WFENCE();
  wait_flags(ufp, uf0, ftag);
  u32x4 v[2], vp;
  {
    const u32 uoff = (u32)((gch * 32 + bi) * 8192 + w * 2048);
    v[0] = ld16_dev(rgu, uoff + l * 16); v[1] = ld16_dev(rgu, uoff + 1024 + l * 16);
    vp = ld16_dev(rgu, w ? uoff - 16 : 0x7ffffff0u);
  }
  SB();
#pragma unroll
  for (int i = 0; i < 16; ++i) sw[i * 68] = ps[i];
#pragma unroll
  for (int h = 0; h < 8; ++h) sw[1156 + (4 * (h >> 1) + (h & 1)) * 68] = ps[16 + h];
  WFENCE();
  bf16x8 a1[4], a2f[4];
#pragma unroll
  for (int ks = 0; ks < 4; ++ks) { a1[ks] = rdf(0, 0, ks); a2f[ks] = rdf(1, 0, ks); }
  SB();
  RT(9);
  SB();
  f32x4 d0 = {0, 0, 0, 0}, d1 = d0, d1s = d0, d2 = d0, d2s = d0;
#pragma unroll
  for (int ks = 0; ks < 4; ++ks) d0 = mfma32(af[ks], cf[ks], d0);
#pragma unroll
  for (int ks = 0; ks < 4; ++ks) {
    d1 = mfma32(a1[ks], cf[ks], d1); d1s = mfma32(a1[ks], cf2[ks], d1s);
    d2 = mfma32(a2f[ks], cf[ks], d2); d2s = mfma32(a2f[ks], cf2[ks], d2s);
  }
  {
    const f32x4 pa = {d2[0], d2[1], d2s[0], d2s[1]}, pb = {d1s[0], d1s[2], 0.f, 0.f};
    LAS f32x4* px = (LAS f32x4*)(sm + B_PX + w * 2048 + l * 16);
    px[0] = mh ? d0 : d1; px[64] = mh ? pa : pb;
    RT(2);
    LBAR();
    LAS f32x4* py = (LAS f32x4*)(sm + B_PX + (w ^ 1) * 2048 + l * 16);
    const f32x4 qa = py[0], qb = py[64];
    LAS char* ka = sm + B_KA + chl * 4608 + 512 + e * 16;
    if (mh == 0) {
      const f32x4 k0 = d0 + qa, k2 = pa + qb;
      *(LAS u32x2*)(ka + (g >> 1) * 256 + ((g & 1) ^ 1) * 8) = (u32x2){pk2(k0[3], k0[2]), pk2(k0[1], k0[0])};
#pragma unroll
      for (int r = 0; r < 2; ++r) {
        const float v_ = k2[r], w_ = k2[2 + r], m_ = (e & 1) ? w_ : v_;
        const u32 vv = pk2(v_, v_);
        *(LAS u32x4*)(ka + 2048 + (2 * g + r) * 256) = (u32x4){pk2(w_, w_), pk2(w_, m_), vv, vv};
      }
    } else {
      const f32x4 k1 = d1 + qa, k1s = pb + qb;
      const bool lo = g < 2;
      const float x_ = lo ? k1[2] : k1[1];
      *(LAS u32x4*)(ka + (lo ? 512 + g * 256 : 1024 + (g - 2) * 512)) =
          (u32x4){pk2(k1s[0], lo ? k1[3] : k1s[0]), pk2(x_, x_), pk2(x_, lo ? k1[1] : k1[0]), pk2(k1[0], k1[0])};
      if (!lo) *(LAS u32x4*)(ka + 1024 + (g - 2) * 512 + 256) = (u32x4){pk2(k1s[1], k1s[1]), pk2(k1[3], k1[3]), pk2(k1[3], k1[2]), pk2(k1[2], k1[2])};
    }
  }
  asm volatile("" :: "v"(vp));
  RT(3);
#pragma unroll
  for (int c = 0; c < 2; ++c) {
    LAS char* uc = sm + B_UC + c * 2 * UC_S + 4 + 28 + (w * 128 + l) * 8;
    u32 carry = vp[2 * c + 1];
#pragma unroll
    for (int i = 0; i < 2; ++i) {
      const u32 lo = v[i][2 * c], hi = v[i][2 * c + 1];
      const u32 prev = __builtin_amdgcn_update_dpp(carry, hi, 0x138  , 0xf, 0xf, false);
      carry = __builtin_amdgcn_readlane(hi, 63);
      *(LAS u32x2*)(uc + UC_S + i * 512) = (u32x2){lo, hi};
      *(LAS u32x2*)(uc + i * 512) = (u32x2){__builtin_amdgcn_alignbit(lo, prev, 16), __builtin_amdgcn_alignbit(hi, lo, 16)};
    }
  }
  LBAR();
  RT(4);
  __builtin_amdgcn_s_setprio(PRIO_B2);
  f32x4 acc[8];
#pragma unroll
  for (int c = 0; c < 8; ++c) acc[c] = (f32x4){0, 0, 0, 0};
  {
    u32 tb[4];
#pragma unroll
    for (int k = 0; k < 4; ++k) {
      tb[k] = (u32)(size_t)sm + B_UC + (chl * 2 + (e & 1)) * UC_S + 4 + (16 * (g >> 1) - 8 * (g & 1) + 8 + (e & ~1)) * 2 + mh * 256 + k * 1024;
      asm volatile("" : "+v"(tb[k]));
    }
    u32 vb = (u32)(size_t)sm + B_KA + chl * 4608 + 512 + (e - (g >> 1)) * 32 + (g & 1) * 16 - mh * 256 - 192;
    asm volatile("" : "+v"(vb));
    u32x2 T[2][8][2]; bf16x8 V[2][8];
    auto rd = [&](auto EE, auto K) {
      constexpr int ee = EE.v, m = K.v / 3, r = K.v % 3;
      if constexpr (r < 2) { LAS u32* tp = (LAS u32*)(size_t)(tb[m >> 1] + ee * 64 + (m & 1) * 512 + r * 8); T[ee & 1][m][r] = (u32x2){tp[0], tp[1]}; }
      else V[ee & 1][m] = *(LAS bf16x8*)(size_t)(vb + (3 - ee) * 64 + m * 512);
    };
    sfor<0, 24>([&](auto K) { rd(IC<0>{}, K); SB(); });
    sfor<0, 4>([&](auto EE) {
      constexpr int ee = EE.v, c = ee & 1;
      sfor<0, 36>([&](auto K) {
        constexpr int k = K.v, mt = CP.t[k], mv = CP.v[k];
        acc[mt + mv] = mfma32(V[c][mv], as8((u32x4){T[c][mt][0][0], T[c][mt][0][1], T[c][mt][1][0], T[c][mt][1][1]}), acc[mt + mv]);
        SB();
        if constexpr (k % 6 == 5) {
          constexpr int n = ee * 6 + k / 6;
          if constexpr (n < 16) dma(IC<32 + n / 2>{}, IC<n & 1>{});
        } else if constexpr (ee < 3 && k - k / 6 >= 4 && k - k / 6 < 28) rd(IC<ee + 1>{}, IC<k - k / 6 - 4>{});
        SB();
      });
    });
  }
  RT(5);
#pragma unroll
  for (int c = 0; c < 8; ++c) *(LAS f32x4*)(sm + B_SCR + (w * 8 + c) * 1024 + l * 16) = acc[c];
  wait_vm<0>();
  LBAR();
  const rsrc_t rout = mkrsrc(p.out, 4u << 20);
#pragma unroll
  for (int k = 0; k < 2; ++k) {
    const int c = 2 * w + k;
    LAS f32x4* xp = (LAS f32x4*)(sm + B_SCR + c * 1024 + l * 16);
    const f32x4 y0 = xp[0] + xp[512], y1 = xp[1024] + xp[1536];
    st16_dev(rout, (u32)(((4 * c + g) * 64 + 2 * bi + (e >> 3)) * 2048 + 1024 + gch * 128 + (e & 7) * 16),
             (u32x4){pk2(y0[0], y1[0]), pk2(y0[1], y1[1]), pk2(y0[2], y1[2]), pk2(y0[3], y1[3])});
  }
  RT(6);
  BAR();
  wait_vm<0>();
  if (l == 0) __hip_atomic_store((GAS u64*)(p.ws + WS_YF) + ((gch * 4 + w) * 32 + bi) * 16, ftag, RLX_AGENT);
  wait_flags((GAS u64*)(p.ws + WS_YF) + ((gch * 4 + (bi >> 3)) * 32 + 4 * w + (l & 3) + (l & 4) * 4) * 16, 0ull, ftag);
  RT(10);
  {
    const u32 off = (u32)((bi * 64 + (t >> 3)) * 2048 + 1024 + gch * 128 + (t & 7) * 16);
    v[0] = ld16_dev(rout, off); v[1] = ld16_dev(rout, off + 32 * 2048);
    LAS char* yp = sm + B_UT + e * 144 + (t >> 4) * 4;
#pragma unroll
    for (int i = 0; i < 2; ++i)
#pragma unroll
      for (int r = 0; r < 4; ++r) *(LAS u32*)(yp + r * 16 * 144 + i * 64) = v[i][r];
    RT(7);
    LBAR();
#pragma unroll
    for (int k = 0; k < 2; ++k) {
      const u32x4 yv = *(LAS u32x4*)(sm + B_UT + ((t >> 3) + 32 * k) * 144 + (t & 7) * 16);
      u32x4 o;
#pragma unroll
      for (int j = 0; j < 4; ++j) o[j] = pk2(bf2f(yv[j]) + bf2f(sk[k][j]), bfhi(yv[j]) + bfhi(sk[k][j]));
      *(u32x4*)((char*)p.out + off + k * 32 * 2048) = o;
    }
  }
  RT(8);
  RTDUMP(0);
  asm volatile(".Lk_code_end_b:");
}

__global__ __launch_bounds__(512, 2) void k_one(Params p) {
  __shared__ __attribute__((aligned(16))) char smem[163840];
  const int blk_id = blockIdx.x;
  const u32 wv = rfl(threadIdx.x >> 6);
  if (wv == 7) {
    u32 la, lb; u64 pc0;
    asm volatile("s_getpc_b64 %0\n.Lk_code_begin:\n\ts_mov_b32 %1, .Lk_code_end_a-.Lk_code_begin\n\ts_mov_b32 %2, .Lk_code_end_b-.Lk_code_begin" : "=s"(pc0), "=s"(la), "=s"(lb));
    const u32 len = la > lb ? la : lb;
    const u32 l = threadIdx.x & 63;
    dma4((u32)(size_t)(LAS char*)smem + BOFF + B_JUNK, mkrsrc((const void*)pc0, len), l < 8 ? ((blk_id >> 3) * 8 + l) * 128 : 0x7ffffff0u);
  }
  if (__builtin_expect(wv < 4, 1)) roleB1(p, (LAS char*)smem + BOFF, blk_id); else roleA1(p, (LAS char*)smem, blk_id);
}

NOPK_POP
extern "C" void kernel_launch(void* const* d_in, const int* in_sizes, int n_in, void* d_out, int out_size, void* d_ws, size_t ws_size,
                              hipStream_t stream) {
  Params p;
  p.x = (const u16*)d_in[0]; p.wqkv = (const u16*)d_in[1]; p.wo = (const u16*)d_in[2]; p.u = (const u16*)d_in[3];
  p.lar = (const u16*)d_in[4]; p.aim = (const u16*)d_in[5]; p.cr = (const u16*)d_in[6]; p.ci = (const u16*)d_in[7];
  p.ldt = (const u16*)d_in[8]; p.dsk = (const u16*)d_in[9]; p.out = (u16*)d_out; p.ws = (char*)d_ws;
  k_one<<<256, 512, 0, stream>>>(p);
}
```
